```python
import torch

def _softmax(x, axis=-1):
    return torch.softmax(x, dim=axis)
def _gelu(x):
    return torch.nn.functional.gelu(x, approximate="tanh")
def _silu(x):
    return torch.nn.functional.silu(x)
def _sigmoid(x):
    return torch.sigmoid(x)
def _layernorm(x, axis=-1, eps=1e-5):
    m = x.mean(dim=axis, keepdim=True)
    v = x.var(dim=axis, keepdim=True, unbiased=False)
    return (x - m) / torch.sqrt(v + eps)
def _scatter_add(tgt, idx, src, axis=0):
    return tgt.clone().index_add_(axis, idx, src)
def _segment_sum(src, seg, n):
    out = torch.zeros((n,) + src.shape[1:], dtype=src.dtype)
    return out.index_add_(0, seg, src)
def _pad(x, pw):
    flat = tuple(v for pair in reversed(pw) for v in pair)
    return torch.nn.functional.pad(x, flat)

kernel_name = 'gated_mix__ssm_conv_s4d__glu_gated_geglu__prompt'

def setup_inputs():

    _w = (lambda a: (lambda t: t.to(torch.bfloat16) if t.is_floating_point() else t)(torch.as_tensor(a)))
    def _A_setup():
        Bsz, L, D, N = 1, 2048, 512, 128
        return {
            "u": _w((lambda *s: torch.randn(*s, dtype=torch.bfloat16) * 0.05)(Bsz, L, D)),
            "log_A_real": _w((lambda *s: torch.randn(*s, dtype=torch.bfloat16) * 6.4)(D, N) * 0.5),
            "A_imag": _w((lambda *s: torch.randn(*s, dtype=torch.bfloat16) * 0.05)(D, N) * 3.14),
            "C_real": _w((lambda *s: torch.randn(*s, dtype=torch.bfloat16) * 0.05)(D, N) / N ** 0.5),
            "C_imag": _w((lambda *s: torch.randn(*s, dtype=torch.bfloat16) * 0.05)(D, N) / N ** 0.5),
            "log_dt": _w((lambda *s: torch.randn(*s, dtype=torch.bfloat16) * 0.05)(D) * 0.5 - 4.0),
            "D_skip": _w((lambda *s: torch.randn(*s, dtype=torch.bfloat16) * 0.05)(D)),
        }
    _A_in = _A_setup()

    def _B_setup():
        B, S, Dm, Df = 1, 2048, 512, 1376
        return {
            "x": _w((lambda *s: torch.randn(*s, dtype=torch.bfloat16) * 0.05)(B, S, Dm)),
            "w_gate": _w((lambda *s: torch.randn(*s, dtype=torch.bfloat16) * 1.6)(Dm, Df) * (Dm ** -0.5)),
            "w_up": _w((lambda *s: torch.randn(*s, dtype=torch.bfloat16) * 1.6)(Dm, Df) * (Dm ** -0.5)),
            "w_down": _w((lambda *s: torch.randn(*s, dtype=torch.bfloat16) * 1.6)(Df, Dm) * (Df ** -0.5)),
        }
    _B_in = _B_setup()

    return {
        "A_u": _A_in["u"],
        "A_log_A_real": _A_in["log_A_real"],
        "A_A_imag": _A_in["A_imag"],
        "A_C_real": _A_in["C_real"],
        "A_C_imag": _A_in["C_imag"],
        "A_log_dt": _A_in["log_dt"],
        "A_D_skip": _A_in["D_skip"],
        "B_x": _B_in["x"],
        "B_w_gate": _B_in["w_gate"],
        "B_w_up": _B_in["w_up"],
        "B_w_down": _B_in["w_down"],
        "gate_w": _w((lambda *s: torch.randn(*s, dtype=torch.bfloat16) * 0.05)(512) * 16.0),
    }

def reference(A_u, A_log_A_real, A_A_imag, A_C_real, A_C_imag, A_log_dt, A_D_skip, B_x, B_w_gate, B_w_up, B_w_down, gate_w):
    def _A_ref(u, log_A_real, A_imag, C_real, C_imag, log_dt, D_skip):
        L = u.shape[1]
        dt = torch.exp(log_dt)
        A = -torch.exp(log_A_real) + 1j * A_imag
        C = (C_real + 1j * C_imag) * dt[:, None]
        pos = (lambda x, dt: x.to(dt))(torch.arange(L), log_dt.dtype)
        P = torch.exp((A * dt[:, None])[:, :, None] * pos[None, None, :])
        K = 2.0 * torch.einsum("dn,dnl->dl", C, P).real
        L2 = 2 * L
        u_t = (lambda x, *d: x.permute(*d))(u, 0, 2, 1)
        Kf = (lambda x, *a, **k: torch.fft.rfft(x.float(), *a, **k))(K, L2)
        Uf = (lambda x, *a, **k: torch.fft.rfft(x.float(), *a, **k))(u_t, L2)
        y = (lambda x, *a, **k: torch.fft.irfft(x, *a, **k).to(torch.bfloat16))(Uf * Kf[None, :, :], L2)[:, :, :L]
        y = (lambda x, *d: x.permute(*d))(y, 0, 2, 1)
        return y + u * D_skip[None, None, :]

    def _B_ref(x, w_gate, w_up, w_down):
        g = torch.einsum("bsd,df->bsf", x, w_gate)
        u = torch.einsum("bsd,df->bsf", x, w_up)
        h = _gelu(g) * u
        return torch.einsum("bsf,fd->bsd", h, w_down)

    x = A_u
    g = _sigmoid((x * gate_w).sum(-1, keepdims=True))
    a = _A_ref(x, A_log_A_real, A_A_imag, A_C_real, A_C_imag, A_log_dt, A_D_skip)
    b = _B_ref(x, B_w_gate, B_w_up, B_w_down)
    return g * a + (1 - g) * b
```

```cpp
#include <hip/hip_runtime.h>
#include <hip/hip_bf16.h>
#include <stdint.h>
typedef unsigned short u16;
__device__ __forceinline__ float bf2f(u16 x){ return __uint_as_float(((unsigned)x)<<16); }
__device__ __forceinline__ u16 f2bf(float f){ unsigned u=__float_as_uint(f); return (u16)((u+0x7FFF+((u>>16)&1))>>16); }
__device__ __forceinline__ float rbf(float f){ return bf2f(f2bf(f)); }
constexpr int L=2048,D=512,N=128,F=1376;
__global__ void kgen(const u16* lar,const u16* aim,const u16* cr,const u16* ci,const u16* ldt,float* K){
  int d=blockIdx.x;
  __shared__ float zr[N],zi[N],c0[N],c1[N];
  float dt=rbf(expf(bf2f(ldt[d])));
  if(threadIdx.x<N){int n=threadIdx.x; zr[n]=-rbf(expf(bf2f(lar[d*N+n])))*dt; zi[n]=bf2f(aim[d*N+n])*dt; c0[n]=bf2f(cr[d*N+n])*dt; c1[n]=bf2f(ci[d*N+n])*dt;}
  __syncthreads();
  for(int l=threadIdx.x;l<L;l+=blockDim.x){ float p=rbf((float)l); float s=0;
    for(int n=0;n<N;n++){ float e=expf(zr[n]*p); float sn,cs; sincosf(zi[n]*p,&sn,&cs); s+=e*(c0[n]*cs-c1[n]*sn);} K[d*L+l]=2.f*s; }
}
__global__ void conv(const u16* u,const float* K,float* y){
  int d=blockIdx.x; __shared__ float ks[L],us[L];
  for(int l=threadIdx.x;l<L;l+=blockDim.x){ks[l]=K[d*L+l];us[l]=bf2f(u[l*D+d]);}
  __syncthreads();
  for(int l=threadIdx.x;l<L;l+=blockDim.x){float s=0;for(int j=0;j<=l;j++) s+=ks[l-j]*us[j]; y[l*D+d]=s;}
}
__global__ void gemm1(const u16* x,const u16* wg,const u16* wu,u16* h){
  int l=blockIdx.x; __shared__ float xs[D];
  for(int i=threadIdx.x;i<D;i+=blockDim.x) xs[i]=bf2f(x[l*D+i]);
  __syncthreads();
  for(int f=threadIdx.x;f<F;f+=blockDim.x){float g=0,uu=0;for(int k=0;k<D;k++){g+=xs[k]*bf2f(wg[k*F+f]);uu+=xs[k]*bf2f(wu[k*F+f]);}
    float t=tanhf(0.7978845608f*(g+0.044715f*g*g*g)); h[l*F+f]=f2bf(0.5f*g*(1.f+t)*uu);}
}
__global__ void gemm2(const u16* x,const u16* h,const u16* wd,const float* y,const u16* dsk,const u16* gw,u16* out){
  int l=blockIdx.x; __shared__ float hs[F]; __shared__ float red[512];
  for(int i=threadIdx.x;i<F;i+=blockDim.x) hs[i]=bf2f(h[l*F+i]);
  int d=threadIdx.x; red[d]=bf2f(x[l*D+d])*bf2f(gw[d]);
  __syncthreads();
  float gs=0; for(int i=0;i<D;i++) gs+=red[i];
  float g=1.f/(1.f+expf(-gs));
  float b=0; for(int k=0;k<F;k++) b+=hs[k]*bf2f(wd[k*D+d]);
  float a=y[l*D+d]+bf2f(x[l*D+d])*bf2f(dsk[d]);
  out[l*D+d]=f2bf(g*a+(1.f-g)*b);
}
extern "C" void kernel_launch(void* const* d_in, const int* in_sizes, int n_in, void* d_out, int out_size, void* d_ws, size_t ws_size, hipStream_t stream){
  const u16* u=(const u16*)d_in[0];
  float* K=(float*)d_ws; float* y=K+D*L; u16* h=(u16*)(y+L*D);
  kgen<<<D,256,0,stream>>>((const u16*)d_in[1],(const u16*)d_in[2],(const u16*)d_in[3],(const u16*)d_in[4],(const u16*)d_in[5],K);
  conv<<<D,256,0,stream>>>(u,K,y);
  gemm1<<<L,256,0,stream>>>(u,(const u16*)d_in[8],(const u16*)d_in[9],h);
  gemm2<<<L,512,0,stream>>>(u,h,(const u16*)d_in[10],y,(const u16*)d_in[6],(const u16*)d_in[11],(u16*)d_out);
}
```

```cpp
#include <hip/hip_runtime.h>
#include <stdint.h>
#pragma clang diagnostic ignored "-Wint-to-pointer-cast"

typedef unsigned long long u64;
typedef __bf16 bf16x8 __attribute__((ext_vector_type(8)));
typedef __bf16 bf16x2 __attribute__((ext_vector_type(2)));
typedef short s16x4 __attribute__((ext_vector_type(4)));
typedef short s16x8 __attribute__((ext_vector_type(8)));
typedef float f32x2 __attribute__((ext_vector_type(2)));
typedef float f32x4 __attribute__((ext_vector_type(4)));
typedef float f32x16 __attribute__((ext_vector_type(16)));
typedef unsigned u32x2 __attribute__((ext_vector_type(2)));
typedef unsigned u32x4 __attribute__((ext_vector_type(4)));
#define LAS __attribute__((address_space(3)))
#define GAS __attribute__((address_space(1)))
#define DI __device__ __forceinline__

constexpr int L_ = 2048, D_ = 512, F_ = 1376;
constexpr unsigned SLOT0 = 0, SLOT1 = 40992, TB0 = 81984, TSZ = 16512, SLOT2 = TB0 + 2 * TSZ, MISC = SLOT2 + 40992;
constexpr unsigned WOFF = 16384, WUP = 12320;
constexpr unsigned UB = SLOT2, WP1 = SLOT2 + 9216, AT = 147456, GWL = MISC + 64;
constexpr unsigned LDS_BYTES = 163840;
constexpr unsigned WS_UF = 4096, WS_HF = 8192, WS_AF = 16384, WS_G = 32768;
constexpr unsigned WS_U = 65536, WS_A = WS_U + 2097152, WS_H = WS_A + 2097152, WS_END = WS_H + 128 * 44032;
constexpr unsigned WS_ST = 16u << 20;

#define STAMP_INIT()
#define STAMP(i)
#define STAMP_END()

DI float bf_lo(unsigned v) { return __uint_as_float(v << 16); }
DI float bf_hi(unsigned v) { return __uint_as_float(v & 0xffff0000u); }
DI unsigned pk(float a, float b) { f32x2 t = {a, b}; return __builtin_bit_cast(unsigned, __builtin_convertvector(t, bf16x2)); }
DI float rbf(float a) { return bf_lo(pk(a, a)); }
DI u32x4 lds128(unsigned a) { return *(LAS u32x4*)a; }
DI u32x2 lds64(unsigned a) { return *(LAS u32x2*)a; }
DI u32x2 ldtr(unsigned a) { return __builtin_bit_cast(u32x2, __builtin_amdgcn_ds_read_tr16_b64_v4i16((LAS s16x4*)a)); }
DI u32x4 ldtr2(unsigned a, unsigned b) { u32x2 x = ldtr(a), y = ldtr(b); return (u32x4){x[0], x[1], y[0], y[1]}; }
DI f32x16 mfma32(u32x4 a, u32x4 b, f32x16 c) { return __builtin_amdgcn_mfma_f32_32x32x16_bf16(__builtin_bit_cast(bf16x8, a), __builtin_bit_cast(bf16x8, b), c, 0, 0, 0); }
DI f32x4 mfma16(u32x4 a, u32x4 b, f32x4 c) { return __builtin_amdgcn_mfma_f32_16x16x32_bf16(__builtin_bit_cast(bf16x8, a), __builtin_bit_cast(bf16x8, b), c, 0, 0, 0); }
DI u32x4 mkrsrc(const void* p, unsigned bytes) { u64 a = (u64)p; return (u32x4){(unsigned)a, (unsigned)(a >> 32) & 0xffffu, bytes, 0x00020000u}; }
DI void dma(unsigned dst, unsigned voff, u32x4 rs, unsigned soff) {
  asm volatile("s_mov_b32 m0, %0\n\ts_nop 0\n\tbuffer_load_dwordx4 %1, %2, %3 offen lds" ::"s"(dst), "v"(voff), "s"(rs), "s"(soff) : "memory");
}
DI void st128(u32x4 v, unsigned voff, u32x4 rs, unsigned soff) {
  asm volatile("buffer_store_dwordx4 %0, %1, %2, %3 offen sc1\n\ts_nop 1" ::"v"(v), "v"(voff), "s"(rs), "s"(soff) : "memory");
}
#define WAITV(n) asm volatile("s_waitcnt vmcnt(" #n ")" ::: "memory")
#define WAITL() asm volatile("s_waitcnt lgkmcnt(0)" ::: "memory")
#define BAR() do { asm volatile("s_waitcnt lgkmcnt(0)" ::: "memory"); __builtin_amdgcn_s_barrier(); asm volatile("" ::: "memory"); } while (0)
#define SBAR() do { __builtin_amdgcn_sched_barrier(0); __builtin_amdgcn_s_barrier(); __builtin_amdgcn_sched_barrier(0); } while (0)
#define RLX __ATOMIC_RELAXED, __HIP_MEMORY_SCOPE_AGENT
DI void poll(const GAS u64* p, u64 tag) {
#pragma unroll 1
  for (unsigned spins = 0; spins < (1u << 16); ++spins) {
    u64 v = __hip_atomic_load(p, RLX);
    if (__all(v == tag)) break;
    __builtin_amdgcn_s_sleep(2);
  }
  asm volatile("" ::: "memory");
}
DI void publish(GAS u64* p, u64 tag, int lane) {
  WAITV(0);
  if (lane == 0) __hip_atomic_store(p, tag, RLX);
}
DI u64 ldtag() { u32x2 t = lds64(MISC); return ((u64)(unsigned)__builtin_amdgcn_readfirstlane(t[1]) << 32) | (unsigned)__builtin_amdgcn_readfirstlane(t[0]); }
DI unsigned slot_of(int k) { int m = k % 3; return m == 0 ? SLOT0 : (m == 1 ? SLOT1 : SLOT2); }

struct cw { f32x2 a, b; };
DI cw mkw(f32x2 w) { return cw{w, (f32x2){-w.y, w.x}}; }
DI f32x2 cmul(f32x2 v, cw w) { return __builtin_elementwise_fma(v.yy, w.b, v.xx * w.a); }
DI f32x2 cexp(float zr2, float zv, float p) {
  float m = __builtin_amdgcn_exp2f(zr2 * p), a = __builtin_amdgcn_fractf(zv * p);
  return (f32x2){m * __builtin_amdgcn_cosf(a), m * __builtin_amdgcn_sinf(a)};
}

template <int TS>
DI void conv_body(unsigned tb, int ch, int lane, f32x4 (&acc)[4]) {
  constexpr int T0 = TS ? 1 : 0, T1 = TS ? 2 : 3, T2 = TS ? 5 : 4, T3 = TS ? 6 : 7;
  const int q = lane >> 4, e = (lane >> 2) & 3, pp = lane & 3, c = lane & 15;
#pragma unroll 1
  for (int r = 0; r < 8; ++r) {
    const int y0 = 8 * r + 8 - 2 * q + pp, y1 = y0 - 1;
    const unsigned a0 = tb + (y0 >> 1) * 64 + (((((y0 & 1) << 1) | (e >> 1)) ^ ((y0 >> 2) & 3)) << 4) + (e & 1) * 8;
    const unsigned a1 = tb + (y1 >> 1) * 64 + (((((y1 & 1) << 1) | (e >> 1)) ^ ((y1 >> 2) & 3)) << 4) + (e & 1) * 8;
    const int o = 30 - 4 * r + 2 * c + q;
    const unsigned ab = UB + o * 32 + ((ch ^ ((o >> 3) & 1)) << 4);
    u32x4 A[T3 + 1], B[T3 + 1];
#pragma unroll
    for (int p = 0; p <= T3; ++p) { A[p] = ldtr2(a0 + p * 2048, a1 + p * 2048); B[p] = lds128(ab + p * 1024); }
#pragma unroll
    for (int p = 0; p <= T3; ++p) {
      if (p <= T0) acc[0] = mfma16(A[p], B[T0 - p < 0 ? 0 : T0 - p], acc[0]);
      if (p <= T1) acc[1] = mfma16(A[p], B[T1 - p < 0 ? 0 : T1 - p], acc[1]);
      if (p <= T2) acc[2] = mfma16(A[p], B[T2 - p < 0 ? 0 : T2 - p], acc[2]);
      acc[3] = mfma16(A[p], B[T3 - p], acc[3]);
    }
  }
}

__global__ __launch_bounds__(512, 2) void fused_kernel(const char* __restrict__ x, const unsigned* __restrict__ lar, const unsigned* __restrict__ aim,
                                                       const unsigned* __restrict__ cre, const unsigned* __restrict__ cim, const unsigned short* __restrict__ ldt,
                                                       const unsigned short* __restrict__ dsk, const char* __restrict__ wg, const char* __restrict__ wu,
                                                       const char* __restrict__ wd, const char* __restrict__ gw, char* __restrict__ out, char* ws) {
  const int b = blockIdx.x, xcd = b & 7, bi = b >> 3;
  const int rt = (xcd & 1) * 8 + (bi & 7), ct = (xcd >> 1) * 4 + (bi >> 3);
  const int m2 = xcd * 4 + (bi & 3), n2 = bi >> 2;
  const int k0 = (bi + (bi >> 3) * 2) & 7;
  const int rot2 = (n2 * 5 + (bi & 3) * 11) % 43;
  const int lane = threadIdx.x & 63, w = __builtin_amdgcn_readfirstlane(threadIdx.x >> 6);
  const bool spare = ct == 15;
  const __amdgpu_buffer_rsrc_t wsr = __builtin_amdgcn_make_buffer_rsrc(ws, 0, WS_END, 0x00020000);
  const u32x4 rwd = mkrsrc(wd, F_ * D_ * 2), rws = mkrsrc(ws, WS_END);
  const unsigned wdv = (lane >> 3) * 1024 + (((lane & 7) ^ (((lane >> 3) & 2) | ((w & 1) << 2))) << 4);
  auto wd_soff = [&](int j, int a) { int ks = j + rot2; ks = ks >= 43 ? ks - 43 : ks; return (unsigned)((32 * ks + 8 * a) * 1024 + n2 * 128); };
  STAMP_INIT(); STAMP(0);

  if (w < 4) {
    const int h = lane >> 5, l31 = lane & 31;
    if (!spare) {
      const int mh = w & 1, nh = w >> 1;
      unsigned xo[4];
#pragma unroll
      for (int s = 0; s < 4; ++s) xo[s] = (64 * mh + l31) * 128 + (((2 * s + h) ^ ((l31 >> 1) & 7)) << 4);
      const unsigned wo = WOFF + ((lane >> 4) & 1) * WUP + (8 * h + ((lane >> 2) & 3)) * 192 + nh * 96 + (lane & 1) * 16 + (lane & 2) * 4;
      f32x16 acc[3][2];
#pragma unroll
      for (int g = 0; g < 3; ++g)
#pragma unroll
        for (int t = 0; t < 2; ++t)
#pragma unroll
          for (int j = 0; j < 16; ++j) acc[g][t][j] = 0.f;
#pragma unroll 1
      for (int k = 0; k < 8; ++k) {
        STAMP(16 + k);
        BAR();
        if (k == 0) STAMP(1);
        const unsigned sb = slot_of(k);
#pragma unroll
        for (int s = 0; s < 4; ++s) {
          u32x4 xb[2], wa[3];
#pragma unroll
          for (int t = 0; t < 2; ++t) xb[t] = lds128(sb + xo[s] + t * 4096);
#pragma unroll
          for (int g = 0; g < 3; ++g) wa[g] = ldtr2(sb + wo + s * 3072 + g * 32, sb + wo + s * 3072 + g * 32 + 768);
#pragma unroll
          for (int g = 0; g < 3; ++g)
#pragma unroll
            for (int t = 0; t < 2; ++t) acc[g][t] = mfma32(wa[g], xb[t], acc[g][t]);
        }
      }
      BAR();
      STAMP(2);
      const unsigned hv = ((lane >> 4) & 1) * 44032 + h * 256 + (lane & 15) * 16;
#pragma unroll
      for (int g = 0; g < 3; ++g) {
        const int g16 = ct * 6 + nh * 3 + g;
#pragma unroll
        for (int t = 0; t < 2; ++t) {
          u32x4 o;
#pragma unroll
          for (int j = 0; j < 4; ++j) {
            const f32x2 gg = {acc[g][t][2 * j], acc[g][t][2 * j + 1]}, uu = {acc[g][t][8 + 2 * j], acc[g][t][9 + 2 * j]};
            const f32x2 z = gg * __builtin_elementwise_fma(gg * gg, (f32x2){-0.1029432397f, -0.1029432397f}, (f32x2){-2.302208198f, -2.302208198f});
            const f32x2 dn = (f32x2){__builtin_amdgcn_exp2f(z.x), __builtin_amdgcn_exp2f(z.y)} + 1.f;
            const f32x2 hh = gg * uu * (f32x2){__builtin_amdgcn_rcpf(dn.x), __builtin_amdgcn_rcpf(dn.y)};
            o[j] = pk(hh.x, hh.y);
          }
          if (g16 < 86) st128(o, hv, rws, WS_H + (rt * 8 + 4 * mh + 2 * t) * 44032 + g16 * 512);
        }
      }
      STAMP(3);
      publish((GAS u64*)(ws + WS_HF) + (2 * rt + mh) * 32 + 2 * ct + nh, ldtag(), lane);
    } else {
      const int g = lane >> 4, e = (lane >> 2) & 3, pp = lane & 3;
      unsigned ta[2][4], xo[4];
#pragma unroll
      for (int q2 = 0; q2 < 2; ++q2)
#pragma unroll
        for (int tt = 0; tt < 4; ++tt)
          ta[q2][tt] = (16 * ((g & 1) * 4 + pp) + e + 4 * tt) * 128 + (((2 * w + q2) ^ (e >> 1) ^ (2 * tt)) << 4) + (g >> 1) * 8;
#pragma unroll
      for (int s = 0; s < 4; ++s) xo[s] = (32 * w + l31) * 128 + (((2 * s + h) ^ ((l31 >> 1) & 7)) << 4);
      const unsigned uv = (g >> 1) * 16384 + (2 * ((g & 1) * 4 + e) + (pp >> 1)) * 32 + (((pp ^ g) & 1) << 4);
      const bool lowp = pp < 2;
      f32x16 ga;
#pragma unroll
      for (int j = 0; j < 16; ++j) ga[j] = 0.f;
#pragma unroll 1
      for (int k = 0; k < 8; ++k) {
        STAMP(16 + k);
        BAR();
        const unsigned sb = slot_of(k);
        const int kt = (k + k0) & 7;
#pragma unroll
        for (int q2 = 0; q2 < 2; ++q2) {
          const u32x4 A = ldtr2(sb + ta[q2][0], sb + ta[q2][1]), B = ldtr2(sb + ta[q2][2], sb + ta[q2][3]);
          u32x4 P0, P1;
#pragma unroll
          for (int j = 0; j < 4; ++j) {
            const unsigned sa = __builtin_amdgcn_mov_dpp(A[j], 0x4E, 0xf, 0xf, true), sbb = __builtin_amdgcn_mov_dpp(B[j], 0x4E, 0xf, 0xf, true);
            P0[j] = lowp ? A[j] : sbb;
            P1[j] = lowp ? sa : B[j];
          }
          const unsigned so = WS_U + (32 * kt + 8 * w + 4 * q2) * 8192 + rt * 512;
          st128(P0, uv, rws, so);
          st128(P1, uv, rws, so + 8192);
        }
#pragma unroll
        for (int s = 0; s < 4; ++s) ga = mfma32(lds128(GWL + kt * 128 + s * 32 + h * 16), lds128(sb + xo[s]), ga);
      }
      const float gate = __builtin_amdgcn_rcpf(1.f + __builtin_amdgcn_exp2f(ga[0] * -1.442695041f));
      if (lane < 32) __hip_atomic_store((GAS float*)(ws + WS_G) + rt * 128 + 32 * w + lane, gate, RLX);
      publish((GAS u64*)(ws + WS_UF) + w * 16 + rt, ldtag(), lane);
      STAMP(2);
      BAR();
    }
    STAMP(4);
#pragma unroll 1
    for (int j = 0; j < 20; ++j) dma(j * 4096 + w * 1024, wdv, rwd, wd_soff(7 + j, w));
    WAITV(0);
    STAMP(5);
    BAR();
    const int q = lane >> 4, e = (lane >> 2) & 3, pp = lane & 3;
    unsigned wa[4];
#pragma unroll
    for (int t4 = 0; t4 < 4; ++t4) wa[t4] = (8 * q + e) * 128 + ((2 * (t4 ^ ((e >> 1) | ((q & 1) << 1))) + (pp >> 1)) << 4) + (pp & 1) * 8;
    u32x4 pre[7][4];
#pragma unroll
    for (int j = 0; j < 7; ++j)
#pragma unroll
      for (int t4 = 0; t4 < 4; ++t4) pre[j][t4] = ldtr2(WP1 + wa[t4] + j * 4096, WP1 + wa[t4] + j * 4096 + 512);
#pragma unroll
    for (int j = 0; j < 7; ++j)
#pragma unroll
      for (int t4 = 0; t4 < 4; ++t4) asm volatile("" : "+v"(pre[j][t4]));
    BAR();
    STAMP(6);
    {
      const GAS u64* fp = lane < 30 ? (const GAS u64*)(ws + WS_HF) + m2 * 32 + lane : (const GAS u64*)(ws + WS_UF) + (2 * (m2 & 1) + (w >> 1)) * 16 + (m2 >> 1);
      poll(fp, ldtag());
    }
    STAMP(7);
    const unsigned hb = WS_H + (m2 * 4 + w) * 44032;
    auto ldh = [&](int j) {
      int ks = j + rot2; ks = ks >= 43 ? ks - 43 : ks;
      return __builtin_amdgcn_raw_buffer_load_b128(wsr, lane * 16, j < 43 ? hb + ks * 1024 : WS_END, 16);
    };
    u32x4 hr[9];
#pragma unroll
    for (int j = 0; j < 9; ++j) hr[(j + 2) % 9] = ldh(j);
    const float gate = __hip_atomic_load((const GAS float*)(ws + WS_G) + m2 * 64 + w * 16 + (lane & 15), RLX);
    f32x4 acc[4];
#pragma unroll
    for (int t4 = 0; t4 < 4; ++t4) acc[t4] = (f32x4){0.f, 0.f, 0.f, 0.f};
#pragma unroll
    for (int j = 0; j < 7; ++j) {
#pragma unroll
      for (int t4 = 0; t4 < 4; ++t4) acc[t4] = mfma16(pre[j][t4], hr[j + 2], acc[t4]);
      hr[j + 2] = ldh(j + 9);
    }
#pragma unroll 1
    for (int it = 0; it < 4; ++it) {
      if (it == 2) BAR();
#pragma unroll
      for (int u = 0; u < 9; ++u) {
#pragma unroll
        for (int t4 = 0; t4 < 4; ++t4) acc[t4] = mfma16(ldtr2(wa[t4] + u * 4096, wa[t4] + u * 4096 + 512), hr[u], acc[t4]);
        hr[u] = ldh(16 + it * 9 + u);
      }
#pragma unroll
      for (int t4 = 0; t4 < 4; ++t4) wa[t4] += 9 * 4096;
    }
    STAMP(8);
    BAR();
    STAMP(9);
    const unsigned st = w * 2304;
#pragma unroll
    for (int t4 = 0; t4 < 4; ++t4) {
      const u32x2 av = ldtr(AT + (16 * t4 + 4 * q + e) * 128 + ((4 * (pp >> 1) + w) << 4) + (pp & 1) * 8);
      const float o0 = acc[t4][0] + gate * (bf_lo(av[0]) - acc[t4][0]), o1 = acc[t4][1] + gate * (bf_hi(av[0]) - acc[t4][1]);
      const float o2 = acc[t4][2] + gate * (bf_lo(av[1]) - acc[t4][2]), o3 = acc[t4][3] + gate * (bf_hi(av[1]) - acc[t4][3]);
      *(LAS u32x2*)(st + (lane & 15) * 144 + t4 * 32 + q * 8) = (u32x2){pk(o0, o1), pk(o2, o3)};
    }
    WAITL();
#pragma unroll
    for (int t = 0; t < 2; ++t) {
      const int row = 8 * t + (lane >> 3);
      *(GAS u32x4*)(out + (size_t)(m2 * 64 + w * 16 + row) * 1024 + n2 * 128 + (lane & 7) * 16) = lds128(st + row * 144 + (lane & 7) * 16);
    }
    STAMP(10);
  } else {
    if (w < 6) {
      const int j = w - 4;
      const u32x4 rx = mkrsrc(x, L_ * D_ * 2), rw = mkrsrc(j ? wu : wg, D_ * F_ * 2);
      unsigned vx[2], vw[3];
#pragma unroll
      for (int a = 0; a < 2; ++a) vx[a] = (lane >> 3) * 1024 + (((lane & 7) ^ ((4 * a + (lane >> 4)) & 7)) << 4);
#pragma unroll
      for (int c = 0; c < 3; ++c) { const unsigned ee = 64 * c + lane; vw[c] = (ee / 12) * 2752 + ((ee % 12) & (ct == 14 ? 3 : 15)) * 16; }
      auto stage = [&](int k) {
        const int kt = (k + k0) & 7;
        const unsigned sb = slot_of(k);
        const unsigned sx = (rt * 128 + 64 * j) * 1024 + kt * 128, sw = kt * 64 * 2752 + ct * 192;
        if (!spare) {
#pragma unroll
          for (int c = 0; c < 12; ++c) dma(sb + WOFF + j * WUP + c * 1024, vw[c % 3], rw, sw + (c / 3) * 16 * 2752);
        }
#pragma unroll
        for (int a = 0; a < 8; ++a) dma(sb + (8 * j + a) * 1024, vx[a & 1], rx, sx + a * 8192);
      };
      if (spare && j == 0) dma(GWL, lane * 16, mkrsrc(gw, 1024), 0);
      stage(0);
#pragma unroll 1
      for (int k = 0; k < 7; ++k) {
        stage(k + 1);
        if (spare) WAITV(8); else WAITV(20);
        STAMP(16 + k);
        __builtin_amdgcn_s_barrier();
      }
#pragma unroll 1
      for (int p = 0; p < 14; ++p) dma(WP1 + (p >> 1) * 4096 + (j + 2 * (p & 1)) * 1024, wdv, rwd, wd_soff(p >> 1, j + 2 * (p & 1)));
      WAITV(14);
      __builtin_amdgcn_s_barrier();
      STAMP(1);
    } else {
      const int d = 2 * b + (w - 6);
      const unsigned tb = TB0 + (w - 6) * TSZ;
      const unsigned p_lar = lar[d * 64 + lane], p_aim = aim[d * 64 + lane], p_cre = cre[d * 64 + lane], p_cim = cim[d * 64 + lane];
      const float ldtv = bf_lo(ldt[d]);
      u64 ep = 0;
      if (w == 7) ep = __hip_atomic_load((const GAS u64*)ws, RLX);
      SBAR();
      if (w == 7) { const u64 tag = ep * 6364136223846793005ull + 1442695040888963407ull; *(LAS u32x2*)MISC = (u32x2){(unsigned)tag, (unsigned)(tag >> 32)}; }
      const float dt = rbf(__builtin_amdgcn_exp2f(ldtv * 1.442695041f));
      const int r31 = lane & 31;
      const u64 gt = (r31 & 16) ? ((r31 & 8) ? 0x001a191600181714ull : 0x15121110130f0e0dull) : ((r31 & 8) ? 0x0c0b0a0a07060504ull : 0x0a09080803020100ull);
      const unsigned ga = tb + (32 + (unsigned)((gt >> (8 * (r31 & 7))) & 31)) * 272 + (lane >> 5) * 16, ea = tb + r31 * 272 + (lane >> 5) * 16;
      const unsigned wb = tb + lane * 4;
      f32x16 c;
#pragma unroll
      for (int j = 0; j < 16; ++j) c[j] = 0.f;
#pragma unroll 1
      for (int ck = 0; ck < 2; ++ck) {
        const int sh = ck ? 0 : 16;
        const float zr2 = -rbf(__builtin_amdgcn_exp2f(bf_hi(p_lar << sh) * 1.442695041f)) * dt * 1.442695041f;
        const float zv = bf_hi(p_aim << sh) * dt * 0.1591549431f;
        const f32x2 w8 = cexp(zr2, zv, 8.f);
        const cw W8 = mkw(w8);
        {
          const cw W32 = mkw(cexp(zr2, zv, 32.f));
          f32x2 ev[4];
          ev[0] = (f32x2){1.f, 0.f}; ev[1] = w8; ev[2] = cmul(w8, W8); ev[3] = cmul(ev[2], W8);
#pragma unroll
          for (int i = 0; i < 32; ++i) {
            *(LAS unsigned*)(wb + i * 272) = pk(ev[i & 3].x, ev[i & 3].y);
            if (i < 28) ev[i & 3] = cmul(ev[i & 3], W32);
          }
        }
        STAMP(17 + 3 * ck);
        SBAR();
        {
          const f32x2 w1 = cexp(zr2, zv, 1.f);
          const cw W1 = mkw(w1), W2 = mkw(cmul(w1, W1)), W4 = mkw(cmul(W2.a, W2)), W256 = mkw(cexp(zr2, zv, 256.f));
          f32x2 g[9];
          g[0] = (f32x2){bf_hi(p_cre << sh), bf_hi(p_cim << sh)} * (2.f * dt);
          g[1] = cmul(g[0], W1); g[2] = cmul(g[0], W2); g[3] = cmul(g[1], W2);
#pragma unroll
          for (int i = 0; i < 4; ++i) g[4 + i] = cmul(g[i], W4);
          g[8] = cmul(g[0], W8);
          int row = 32;
#pragma unroll
          for (int i = 0; i < 8; ++i) *(LAS unsigned*)(wb + (row++) * 272) = pk(g[i].x, -g[i].y);
#pragma unroll
          for (int bk = 1; bk < 8; ++bk) {
            const int stp = bk == 1 ? 2 : (bk < 4 ? 4 : 8);
#pragma unroll
            for (int i = 0; i <= 8; i += stp) { g[i] = cmul(g[i], W256); *(LAS unsigned*)(wb + (row++) * 272) = pk(g[i].x, -g[i].y); }
          }
        }
        STAMP(18 + 3 * ck);
        BAR();
#pragma unroll
        for (int s = 0; s < 8; ++s) c = mfma32(lds128(ga + s * 32), lds128(ea + s * 32), c);
        STAMP(19 + 3 * ck);
        BAR();
      }
      unsigned pe3 = 0, po2 = 0;
      const unsigned tw = tb + 128 + lane * 64, sx = (((lane >> 1) + 1) & 3) << 4;
      if (lane < 8) *(LAS u32x4*)(tb + lane * 16) = (u32x4){0, 0, 0, 0};
#pragma unroll
      for (int st = 0; st < 4; ++st) {
        float k[8];
        if (st == 0) {
#pragma unroll
          for (int r = 0; r < 8; ++r) k[r] = c[r];
        } else if (st == 1) {
          k[0] = k[1] = k[2] = c[8]; k[3] = k[4] = k[5] = c[9]; k[6] = k[7] = c[10];
        } else {
          const float lo = c[7 + 2 * st], hi = c[8 + 2 * st];
          k[0] = k[1] = k[2] = k[3] = lo; k[4] = (lane & 1) ? hi : lo; k[5] = k[6] = k[7] = hi;
        }
        const unsigned e0 = pk(k[0], k[1]), e1 = pk(k[2], k[3]), e2 = pk(k[4], k[5]), e3 = pk(k[6], k[7]);
        const unsigned o0 = pk(k[1], k[2]), o1 = pk(k[3], k[4]), o2 = pk(k[5], k[6]);
        const unsigned pe = __builtin_amdgcn_update_dpp(pe3, e3, 0x138, 0xf, 0xf, false), po = __builtin_amdgcn_update_dpp(po2, o2, 0x138, 0xf, 0xf, false);
        pe3 = __builtin_amdgcn_readlane(e3, 63); po2 = __builtin_amdgcn_readlane(o2, 63);
        const unsigned om = __builtin_amdgcn_alignbit(e0, pe, 16);
        *(LAS u32x4*)(tw + st * 4096 + (0 ^ sx)) = (u32x4){e0, e1, om, o0};
        *(LAS u32x4*)(tw + st * 4096 + (16 ^ sx)) = (u32x4){pe, e0, po, om};
        *(LAS u32x4*)(tw + st * 4096 + (32 ^ sx)) = (u32x4){e2, e3, o1, o2};
        *(LAS u32x4*)(tw + st * 4096 + (48 ^ sx)) = (u32x4){e1, e2, o0, o1};
      }
      STAMP(1);
      BAR();
      poll((const GAS u64*)(ws + WS_UF) + ((b >> 3) & 3) * 16 + (lane & 15), ldtag());
      STAMP(2);
      u32x4 uu[4];
#pragma unroll
      for (int j = 0; j < 4; ++j) uu[j] = __builtin_amdgcn_raw_buffer_load_b128(wsr, lane * 16, WS_U + b * 8192 + ((w - 6) * 4 + j) * 1024, 16);
      if (w == 6) *(LAS u32x4*)(UB + lane * 16) = (u32x4){0, 0, 0, 0};
#pragma unroll
      for (int j = 0; j < 4; ++j) *(LAS u32x4*)(UB + 1024 + ((w - 6) * 4 + j) * 1024 + lane * 16) = uu[j];
    }
    BAR();
    STAMP(3);
    const int ch = w & 1, ts = (w - 4) >> 1, d = 2 * b + ch;
    const float dskip = bf_lo(dsk[d]);
    f32x4 acc[4];
#pragma unroll
    for (int t = 0; t < 4; ++t) acc[t] = (f32x4){0.f, 0.f, 0.f, 0.f};
    if (ts) conv_body<1>(TB0 + ch * TSZ, ch, lane, acc); else conv_body<0>(TB0 + ch * TSZ, ch, lane, acc);
    STAMP(4);
    {
      const int q = lane >> 4, c = lane & 15;
      const int o = 32 + 2 * c + (q >> 1);
      const unsigned ua = UB + o * 32 + ((ch ^ ((o >> 3) & 1)) << 4) + (q & 1) * 8;
      u32x2 pa[4];
#pragma unroll
      for (int t = 0; t < 4; ++t) {
        const int tt = 2 * t + ((t ^ ts) & 1);
        const u32x2 uv = lds64(ua + tt * 1024);
        pa[t] = (u32x2){pk(acc[t][0] + bf_lo(uv[0]) * dskip, acc[t][1] + bf_hi(uv[0]) * dskip), pk(acc[t][2] + bf_lo(uv[1]) * dskip, acc[t][3] + bf_hi(uv[1]) * dskip)};
      }
      const int tl = (q & 1) ? 3 - ts : ts;
      const unsigned av = (4 * tl + (c >> 2)) * 128 + ((4 * (q >> 1) + (c & 3)) << 4);
#pragma unroll
      for (int t = 0; t < 2; ++t) {
        const auto s0 = __builtin_amdgcn_permlane16_swap(pa[2 * t][0], pa[2 * t + 1][0], false, false);
        const auto s1 = __builtin_amdgcn_permlane16_swap(pa[2 * t][1], pa[2 * t + 1][1], false, false);
        st128((u32x4){s0[0], s1[0], s0[1], s1[1]}, av, rws, WS_A + d * 4096 + t * 2048);
      }
    }
    const u64 tag = ldtag();
    publish((GAS u64*)(ws + WS_AF) + ts * 512 + d, tag, lane);
    STAMP(5);
    BAR();
    const int a = w - 4;
#pragma unroll 1
    for (int j = 20; j < 28; ++j) dma(j * 4096 + a * 1024, wdv, rwd, wd_soff(7 + j, a));
    __builtin_amdgcn_s_barrier();
#pragma unroll 1
    for (int j = 28; j < 36; ++j) dma(j * 4096 + a * 1024, wdv, rwd, wd_soff(7 + j, a));
    WAITV(0);
    STAMP(6);
    __builtin_amdgcn_s_barrier();
    {
      const int t = m2 >> 2, tsn = (t ^ (t >> 1)) & 1;
      poll((const GAS u64*)(ws + WS_AF) + tsn * 512 + n2 * 64 + a * 16 + (lane & 15), tag);
      STAMP(7);
      u32x4 av[2];
#pragma unroll
      for (int j = 0; j < 2; ++j) av[j] = __builtin_amdgcn_raw_buffer_load_b128(wsr, (lane >> 3) * 4096 + (lane & 7) * 16, WS_A + (n2 * 64 + a * 16 + j * 8) * 4096 + m2 * 128, 16);
#pragma unroll
      for (int j = 0; j < 2; ++j) *(LAS u32x4*)(AT + (a * 2 + j) * 1024 + lane * 16) = av[j];
    }
    BAR();
    if (b == 0 && w == 4) {
#pragma unroll 1
      for (int j = 0; j < 4; ++j) poll((const GAS u64*)(ws + WS_AF) + 2 * (j * 64 + lane), tag);
      if (lane == 0) __hip_atomic_store((GAS u64*)ws, tag, RLX);
    }
  }
  STAMP_END();
}

extern "C" void kernel_launch(void* const* d_in, const int* in_sizes, int n_in, void* d_out, int out_size, void* d_ws, size_t ws_size,
                              hipStream_t stream) {
  (void)hipFuncSetAttribute((const void*)fused_kernel, hipFuncAttributeMaxDynamicSharedMemorySize, LDS_BYTES);
  fused_kernel<<<256, 512, LDS_BYTES, stream>>>((const char*)d_in[0], (const unsigned*)d_in[1], (const unsigned*)d_in[2], (const unsigned*)d_in[3],
                                                (const unsigned*)d_in[4], (const unsigned short*)d_in[5], (const unsigned short*)d_in[6],
                                                (const char*)d_in[8], (const char*)d_in[9], (const char*)d_in[10], (const char*)d_in[11],
                                                (char*)d_out, (char*)d_ws);
}
```

```cpp
#include <hip/hip_runtime.h>
#include <stdint.h>
#pragma clang diagnostic ignored "-Wint-to-pointer-cast"

typedef unsigned long long u64;
typedef __bf16 bf16x8 __attribute__((ext_vector_type(8)));
typedef __bf16 bf16x2 __attribute__((ext_vector_type(2)));
typedef short s16x4 __attribute__((ext_vector_type(4)));
typedef short s16x8 __attribute__((ext_vector_type(8)));
typedef float f32x2 __attribute__((ext_vector_type(2)));
typedef float f32x4 __attribute__((ext_vector_type(4)));
typedef float f32x16 __attribute__((ext_vector_type(16)));
typedef unsigned u32x2 __attribute__((ext_vector_type(2)));
typedef unsigned u32x4 __attribute__((ext_vector_type(4)));
#define LAS __attribute__((address_space(3)))
#define GAS __attribute__((address_space(1)))
#define DI __device__ __forceinline__

constexpr int L_ = 2048, D_ = 512, F_ = 1376;
constexpr unsigned SLOT0 = 0, SLOT1 = 40992, TB0 = 81984, TSZ = 16512, SLOT2 = TB0 + 2 * TSZ, MISC = SLOT2 + 40992;
constexpr unsigned WOFF = 16384, WUP = 12320;
constexpr unsigned UB = SLOT2, WP1 = SLOT2 + 9216, AT = 147456, GWL = MISC + 64;
constexpr unsigned LDS_BYTES = 163840;
constexpr unsigned WS_UF = 4096, WS_HF = 8192, WS_AF = 16384, WS_G = 32768;
constexpr unsigned WS_U = 65536, WS_A = WS_U + 2097152, WS_H = WS_A + 2097152, WS_END = WS_H + 128 * 44032;
constexpr unsigned WS_ST = 16u << 20;

#define STAMP_INIT() const u64 pr_r0 = __builtin_amdgcn_s_memrealtime(); const u64 pr_c0 = __builtin_amdgcn_s_memtime(); u64 pr_t = 0;
#define STAMP(i) do { if ((i) == -1) pr_t = __builtin_amdgcn_s_memrealtime() - pr_r0; } while (0)
#define STAMP_END() do { STAMP(61); if (w == 0) { while (__builtin_amdgcn_s_memrealtime() - pr_r0 < 5000 + 4 * pr_t) __builtin_amdgcn_s_sleep(1); } } while (0)


DI float bf_lo(unsigned v) { return __uint_as_float(v << 16); }
DI float bf_hi(unsigned v) { return __uint_as_float(v & 0xffff0000u); }
DI unsigned pk(float a, float b) { f32x2 t = {a, b}; return __builtin_bit_cast(unsigned, __builtin_convertvector(t, bf16x2)); }
DI float rbf(float a) { return bf_lo(pk(a, a)); }
DI u32x4 lds128(unsigned a) { return *(LAS u32x4*)a; }
DI u32x2 lds64(unsigned a) { return *(LAS u32x2*)a; }
DI u32x2 ldtr(unsigned a) { return __builtin_bit_cast(u32x2, __builtin_amdgcn_ds_read_tr16_b64_v4i16((LAS s16x4*)a)); }
DI u32x4 ldtr2(unsigned a, unsigned b) { u32x2 x = ldtr(a), y = ldtr(b); return (u32x4){x[0], x[1], y[0], y[1]}; }
DI f32x16 mfma32(u32x4 a, u32x4 b, f32x16 c) { return __builtin_amdgcn_mfma_f32_32x32x16_bf16(__builtin_bit_cast(bf16x8, a), __builtin_bit_cast(bf16x8, b), c, 0, 0, 0); }
DI f32x4 mfma16(u32x4 a, u32x4 b, f32x4 c) { return __builtin_amdgcn_mfma_f32_16x16x32_bf16(__builtin_bit_cast(bf16x8, a), __builtin_bit_cast(bf16x8, b), c, 0, 0, 0); }
DI u32x4 mkrsrc(const void* p, unsigned bytes) { u64 a = (u64)p; return (u32x4){(unsigned)a, (unsigned)(a >> 32) & 0xffffu, bytes, 0x00020000u}; }
DI void dma(unsigned dst, unsigned voff, u32x4 rs, unsigned soff) {
  asm volatile("s_mov_b32 m0, %0\n\ts_nop 0\n\tbuffer_load_dwordx4 %1, %2, %3 offen lds" ::"s"(dst), "v"(voff), "s"(rs), "s"(soff) : "memory");
}
DI void st128(u32x4 v, unsigned voff, u32x4 rs, unsigned soff) {
  asm volatile("buffer_store_dwordx4 %0, %1, %2, %3 offen sc1\n\ts_nop 1" ::"v"(v), "v"(voff), "s"(rs), "s"(soff) : "memory");
}
#define WAITV(n) asm volatile("s_waitcnt vmcnt(" #n ")" ::: "memory")
#define WAITL() asm volatile("s_waitcnt lgkmcnt(0)" ::: "memory")
#define BAR() do { asm volatile("s_waitcnt lgkmcnt(0)" ::: "memory"); __builtin_amdgcn_s_barrier(); asm volatile("" ::: "memory"); } while (0)
#define SBAR() do { __builtin_amdgcn_sched_barrier(0); __builtin_amdgcn_s_barrier(); __builtin_amdgcn_sched_barrier(0); } while (0)
#define RLX __ATOMIC_RELAXED, __HIP_MEMORY_SCOPE_AGENT
DI void poll(const GAS u64* p, u64 tag) {
#pragma unroll 1
  for (unsigned spins = 0; spins < (1u << 16); ++spins) {
    u64 v = __hip_atomic_load(p, RLX);
    if (__all(v == tag)) break;
    __builtin_amdgcn_s_sleep(2);
  }
  asm volatile("" ::: "memory");
}
DI void publish(GAS u64* p, u64 tag, int lane) {
  WAITV(0);
  if (lane == 0) __hip_atomic_store(p, tag, RLX);
}
DI u64 ldtag() { u32x2 t = lds64(MISC); return ((u64)(unsigned)__builtin_amdgcn_readfirstlane(t[1]) << 32) | (unsigned)__builtin_amdgcn_readfirstlane(t[0]); }
DI unsigned slot_of(int k) { int m = k % 3; return m == 0 ? SLOT0 : (m == 1 ? SLOT1 : SLOT2); }

struct cw { f32x2 a, b; };
DI cw mkw(f32x2 w) { return cw{w, (f32x2){-w.y, w.x}}; }
DI f32x2 cmul(f32x2 v, cw w) { return __builtin_elementwise_fma(v.yy, w.b, v.xx * w.a); }
DI f32x2 cexp(float zr2, float zv, float p) {
  float m = __builtin_amdgcn_exp2f(zr2 * p), a = __builtin_amdgcn_fractf(zv * p);
  return (f32x2){m * __builtin_amdgcn_cosf(a), m * __builtin_amdgcn_sinf(a)};
}

template <int TS>
DI void conv_body(unsigned tb, int ch, int lane, f32x4 (&acc)[4]) {
  constexpr int T0 = TS ? 1 : 0, T1 = TS ? 2 : 3, T2 = TS ? 5 : 4, T3 = TS ? 6 : 7;
  const int q = lane >> 4, e = (lane >> 2) & 3, pp = lane & 3, c = lane & 15;
#pragma unroll 1
  for (int r = 0; r < 8; ++r) {
    const int y0 = 8 * r + 8 - 2 * q + pp, y1 = y0 - 1;
    const unsigned a0 = tb + (y0 >> 1) * 64 + (((((y0 & 1) << 1) | (e >> 1)) ^ ((y0 >> 2) & 3)) << 4) + (e & 1) * 8;
    const unsigned a1 = tb + (y1 >> 1) * 64 + (((((y1 & 1) << 1) | (e >> 1)) ^ ((y1 >> 2) & 3)) << 4) + (e & 1) * 8;
    const int o = 30 - 4 * r + 2 * c + q;
    const unsigned ab = UB + o * 32 + ((ch ^ ((o >> 3) & 1)) << 4);
    u32x4 A[T3 + 1], B[T3 + 1];
#pragma unroll
    for (int p = 0; p <= T3; ++p) { A[p] = ldtr2(a0 + p * 2048, a1 + p * 2048); B[p] = lds128(ab + p * 1024); }
#pragma unroll
    for (int p = 0; p <= T3; ++p) {
      if (p <= T0) acc[0] = mfma16(A[p], B[T0 - p < 0 ? 0 : T0 - p], acc[0]);
      if (p <= T1) acc[1] = mfma16(A[p], B[T1 - p < 0 ? 0 : T1 - p], acc[1]);
      if (p <= T2) acc[2] = mfma16(A[p], B[T2 - p < 0 ? 0 : T2 - p], acc[2]);
      acc[3] = mfma16(A[p], B[T3 - p], acc[3]);
    }
  }
}

__global__ __launch_bounds__(512, 2) void fused_kernel(const char* __restrict__ x, const unsigned* __restrict__ lar, const unsigned* __restrict__ aim,
                                                       const unsigned* __restrict__ cre, const unsigned* __restrict__ cim, const unsigned short* __restrict__ ldt,
                                                       const unsigned short* __restrict__ dsk, const char* __restrict__ wg, const char* __restrict__ wu,
                                                       const char* __restrict__ wd, const char* __restrict__ gw, char* __restrict__ out, char* ws) {
  const int b = blockIdx.x, xcd = b & 7, bi = b >> 3;
  const int rt = (xcd & 1) * 8 + (bi & 7), ct = (xcd >> 1) * 4 + (bi >> 3);
  const int m2 = xcd * 4 + (bi & 3), n2 = bi >> 2;
  const int k0 = (bi + (bi >> 3) * 2) & 7;
  const int rot2 = (n2 * 5 + (bi & 3) * 11) % 43;
  const int lane = threadIdx.x & 63, w = __builtin_amdgcn_readfirstlane(threadIdx.x >> 6);
  const bool spare = ct == 15;
  const __amdgpu_buffer_rsrc_t wsr = __builtin_amdgcn_make_buffer_rsrc(ws, 0, WS_END, 0x00020000);
  const u32x4 rwd = mkrsrc(wd, F_ * D_ * 2), rws = mkrsrc(ws, WS_END);
  const unsigned wdv = (lane >> 3) * 1024 + (((lane & 7) ^ (((lane >> 3) & 2) | ((w & 1) << 2))) << 4);
  auto wd_soff = [&](int j, int a) { int ks = j + rot2; ks = ks >= 43 ? ks - 43 : ks; return (unsigned)((32 * ks + 8 * a) * 1024 + n2 * 128); };
  STAMP_INIT(); STAMP(0);

  if (w < 4) {
    const int h = lane >> 5, l31 = lane & 31;
    if (!spare) {
      const int mh = w & 1, nh = w >> 1;
      unsigned xo[4];
#pragma unroll
      for (int s = 0; s < 4; ++s) xo[s] = (64 * mh + l31) * 128 + (((2 * s + h) ^ ((l31 >> 1) & 7)) << 4);
      const unsigned wo = WOFF + ((lane >> 4) & 1) * WUP + (8 * h + ((lane >> 2) & 3)) * 192 + nh * 96 + (lane & 1) * 16 + (lane & 2) * 4;
      f32x16 acc[3][2];
#pragma unroll
      for (int g = 0; g < 3; ++g)
#pragma unroll
        for (int t = 0; t < 2; ++t)
#pragma unroll
          for (int j = 0; j < 16; ++j) acc[g][t][j] = 0.f;
#pragma unroll 1
      for (int k = 0; k < 8; ++k) {
        STAMP(16 + k);
        BAR();
        if (k == 0) STAMP(1);
        const unsigned sb = slot_of(k);
#pragma unroll
        for (int s = 0; s < 4; ++s) {
          u32x4 xb[2], wa[3];
#pragma unroll
          for (int t = 0; t < 2; ++t) xb[t] = lds128(sb + xo[s] + t * 4096);
#pragma unroll
          for (int g = 0; g < 3; ++g) wa[g] = ldtr2(sb + wo + s * 3072 + g * 32, sb + wo + s * 3072 + g * 32 + 768);
#pragma unroll
          for (int g = 0; g < 3; ++g)
#pragma unroll
            for (int t = 0; t < 2; ++t) acc[g][t] = mfma32(wa[g], xb[t], acc[g][t]);
        }
      }
      BAR();
      STAMP(2);
      const unsigned hv = ((lane >> 4) & 1) * 44032 + h * 256 + (lane & 15) * 16;
#pragma unroll
      for (int g = 0; g < 3; ++g) {
        const int g16 = ct * 6 + nh * 3 + g;
#pragma unroll
        for (int t = 0; t < 2; ++t) {
          u32x4 o;
#pragma unroll
          for (int j = 0; j < 4; ++j) {
            const f32x2 gg = {acc[g][t][2 * j], acc[g][t][2 * j + 1]}, uu = {acc[g][t][8 + 2 * j], acc[g][t][9 + 2 * j]};
            const f32x2 z = gg * __builtin_elementwise_fma(gg * gg, (f32x2){-0.1029432397f, -0.1029432397f}, (f32x2){-2.302208198f, -2.302208198f});
            const f32x2 dn = (f32x2){__builtin_amdgcn_exp2f(z.x), __builtin_amdgcn_exp2f(z.y)} + 1.f;
            const f32x2 hh = gg * uu * (f32x2){__builtin_amdgcn_rcpf(dn.x), __builtin_amdgcn_rcpf(dn.y)};
            o[j] = pk(hh.x, hh.y);
          }
          if (g16 < 86) st128(o, hv, rws, WS_H + (rt * 8 + 4 * mh + 2 * t) * 44032 + g16 * 512);
        }
      }
      STAMP(3);
      publish((GAS u64*)(ws + WS_HF) + (2 * rt + mh) * 32 + 2 * ct + nh, ldtag(), lane);
    } else {
      const int g = lane >> 4, e = (lane >> 2) & 3, pp = lane & 3;
      unsigned ta[2][4], xo[4];
#pragma unroll
      for (int q2 = 0; q2 < 2; ++q2)
#pragma unroll
        for (int tt = 0; tt < 4; ++tt)
          ta[q2][tt] = (16 * ((g & 1) * 4 + pp) + e + 4 * tt) * 128 + (((2 * w + q2) ^ (e >> 1) ^ (2 * tt)) << 4) + (g >> 1) * 8;
#pragma unroll
      for (int s = 0; s < 4; ++s) xo[s] = (32 * w + l31) * 128 + (((2 * s + h) ^ ((l31 >> 1) & 7)) << 4);
      const unsigned uv = (g >> 1) * 16384 + (2 * ((g & 1) * 4 + e) + (pp >> 1)) * 32 + (((pp ^ g) & 1) << 4);
      const bool lowp = pp < 2;
      f32x16 ga;
#pragma unroll
      for (int j = 0; j < 16; ++j) ga[j] = 0.f;
#pragma unroll 1
      for (int k = 0; k < 8; ++k) {
        STAMP(16 + k);
        BAR();
        const unsigned sb = slot_of(k);
        const int kt = (k + k0) & 7;
#pragma unroll
        for (int q2 = 0; q2 < 2; ++q2) {
          const u32x4 A = ldtr2(sb + ta[q2][0], sb + ta[q2][1]), B = ldtr2(sb + ta[q2][2], sb + ta[q2][3]);
          u32x4 P0, P1;
#pragma unroll
          for (int j = 0; j < 4; ++j) {
            const unsigned sa = __builtin_amdgcn_mov_dpp(A[j], 0x4E, 0xf, 0xf, true), sbb = __builtin_amdgcn_mov_dpp(B[j], 0x4E, 0xf, 0xf, true);
            P0[j] = lowp ? A[j] : sbb;
            P1[j] = lowp ? sa : B[j];
          }
          const unsigned so = WS_U + (32 * kt + 8 * w + 4 * q2) * 8192 + rt * 512;
          st128(P0, uv, rws, so);
          st128(P1, uv, rws, so + 8192);
        }
#pragma unroll
        for (int s = 0; s < 4; ++s) ga = mfma32(lds128(GWL + kt * 128 + s * 32 + h * 16), lds128(sb + xo[s]), ga);
      }
      const float gate = __builtin_amdgcn_rcpf(1.f + __builtin_amdgcn_exp2f(ga[0] * -1.442695041f));
      if (lane < 32) __hip_atomic_store((GAS float*)(ws + WS_G) + rt * 128 + 32 * w + lane, gate, RLX);
      publish((GAS u64*)(ws + WS_UF) + w * 16 + rt, ldtag(), lane);
      STAMP(2);
      BAR();
    }
    STAMP(4);
#pragma unroll 1
    for (int j = 0; j < 20; ++j) dma(j * 4096 + w * 1024, wdv, rwd, wd_soff(7 + j, w));
    WAITV(0);
    STAMP(5);
    BAR();
    const int q = lane >> 4, e = (lane >> 2) & 3, pp = lane & 3;
    unsigned wa[4];
#pragma unroll
    for (int t4 = 0; t4 < 4; ++t4) wa[t4] = (8 * q + e) * 128 + ((2 * (t4 ^ ((e >> 1) | ((q & 1) << 1))) + (pp >> 1)) << 4) + (pp & 1) * 8;
    u32x4 pre[7][4];
#pragma unroll
    for (int j = 0; j < 7; ++j)
#pragma unroll
      for (int t4 = 0; t4 < 4; ++t4) pre[j][t4] = ldtr2(WP1 + wa[t4] + j * 4096, WP1 + wa[t4] + j * 4096 + 512);
#pragma unroll
    for (int j = 0; j < 7; ++j)
#pragma unroll
      for (int t4 = 0; t4 < 4; ++t4) asm volatile("" : "+v"(pre[j][t4]));
    BAR();
    STAMP(6);
    {
      const GAS u64* fp = lane < 30 ? (const GAS u64*)(ws + WS_HF) + m2 * 32 + lane : (const GAS u64*)(ws + WS_UF) + (2 * (m2 & 1) + (w >> 1)) * 16 + (m2 >> 1);
      poll(fp, ldtag());
    }
    STAMP(7);
    const unsigned hb = WS_H + (m2 * 4 + w) * 44032;
    auto ldh = [&](int j) {
      int ks = j + rot2; ks = ks >= 43 ? ks - 43 : ks;
      return __builtin_amdgcn_raw_buffer_load_b128(wsr, lane * 16, j < 43 ? hb + ks * 1024 : WS_END, 16);
    };
    u32x4 hr[9];
#pragma unroll
    for (int j = 0; j < 9; ++j) hr[(j + 2) % 9] = ldh(j);
    const float gate = __hip_atomic_load((const GAS float*)(ws + WS_G) + m2 * 64 + w * 16 + (lane & 15), RLX);
    f32x4 acc[4];
#pragma unroll
    for (int t4 = 0; t4 < 4; ++t4) acc[t4] = (f32x4){0.f, 0.f, 0.f, 0.f};
#pragma unroll
    for (int j = 0; j < 7; ++j) {
#pragma unroll
      for (int t4 = 0; t4 < 4; ++t4) acc[t4] = mfma16(pre[j][t4], hr[j + 2], acc[t4]);
      hr[j + 2] = ldh(j + 9);
    }
#pragma unroll 1
    for (int it = 0; it < 4; ++it) {
      if (it == 2) BAR();
#pragma unroll
      for (int u = 0; u < 9; ++u) {
#pragma unroll
        for (int t4 = 0; t4 < 4; ++t4) acc[t4] = mfma16(ldtr2(wa[t4] + u * 4096, wa[t4] + u * 4096 + 512), hr[u], acc[t4]);
        hr[u] = ldh(16 + it * 9 + u);
      }
#pragma unroll
      for (int t4 = 0; t4 < 4; ++t4) wa[t4] += 9 * 4096;
    }
    STAMP(8);
    BAR();
    STAMP(9);
    const unsigned st = w * 2304;
#pragma unroll
    for (int t4 = 0; t4 < 4; ++t4) {
      const u32x2 av = ldtr(AT + (16 * t4 + 4 * q + e) * 128 + ((4 * (pp >> 1) + w) << 4) + (pp & 1) * 8);
      const float o0 = acc[t4][0] + gate * (bf_lo(av[0]) - acc[t4][0]), o1 = acc[t4][1] + gate * (bf_hi(av[0]) - acc[t4][1]);
      const float o2 = acc[t4][2] + gate * (bf_lo(av[1]) - acc[t4][2]), o3 = acc[t4][3] + gate * (bf_hi(av[1]) - acc[t4][3]);
      *(LAS u32x2*)(st + (lane & 15) * 144 + t4 * 32 + q * 8) = (u32x2){pk(o0, o1), pk(o2, o3)};
    }
    WAITL();
#pragma unroll
    for (int t = 0; t < 2; ++t) {
      const int row = 8 * t + (lane >> 3);
      st128(lds128(st + row * 144 + (lane & 7) * 16), row * 1024 + (lane & 7) * 16, mkrsrc(out, L_ * D_ * 2), (m2 * 64 + w * 16) * 1024 + n2 * 128);
    }
    STAMP(10);
  } else {
    if (w < 6) {
      const int j = w - 4;
      const u32x4 rx = mkrsrc(x, L_ * D_ * 2), rw = mkrsrc(j ? wu : wg, D_ * F_ * 2);
      unsigned vx[2], vw[3];
#pragma unroll
      for (int a = 0; a < 2; ++a) vx[a] = (lane >> 3) * 1024 + (((lane & 7) ^ ((4 * a + (lane >> 4)) & 7)) << 4);
#pragma unroll
      for (int c = 0; c < 3; ++c) { const unsigned ee = 64 * c + lane; vw[c] = (ee / 12) * 2752 + ((ee % 12) & (ct == 14 ? 3 : 15)) * 16; }
      auto stage = [&](int k) {
        const int kt = (k + k0) & 7;
        const unsigned sb = slot_of(k);
        const unsigned sx = (rt * 128 + 64 * j) * 1024 + kt * 128, sw = kt * 64 * 2752 + ct * 192;
        if (!spare) {
#pragma unroll
          for (int c = 0; c < 12; ++c) dma(sb + WOFF + j * WUP + c * 1024, vw[c % 3], rw, sw + (c / 3) * 16 * 2752);
        }
#pragma unroll
        for (int a = 0; a < 8; ++a) dma(sb + (8 * j + a) * 1024, vx[a & 1], rx, sx + a * 8192);
      };
      if (spare && j == 0) dma(GWL, lane * 16, mkrsrc(gw, 1024), 0);
      stage(0);
#pragma unroll 1
      for (int k = 0; k < 7; ++k) {
        stage(k + 1);
        if (spare) WAITV(8); else WAITV(20);
        STAMP(16 + k);
        __builtin_amdgcn_s_barrier();
      }
#pragma unroll 1
      for (int p = 0; p < 14; ++p) dma(WP1 + (p >> 1) * 4096 + (j + 2 * (p & 1)) * 1024, wdv, rwd, wd_soff(p >> 1, j + 2 * (p & 1)));
      WAITV(14);
      __builtin_amdgcn_s_barrier();
      STAMP(1);
    } else {
      const int d = 2 * b + (w - 6);
      const unsigned tb = TB0 + (w - 6) * TSZ;
      const unsigned p_lar = lar[d * 64 + lane], p_aim = aim[d * 64 + lane], p_cre = cre[d * 64 + lane], p_cim = cim[d * 64 + lane];
      const float ldtv = bf_lo(ldt[d]);
      u64 ep = 0;
      if (w == 7) ep = __hip_atomic_load((const GAS u64*)ws, RLX);
      SBAR();
      if (w == 7) { const u64 tag = ep * 6364136223846793005ull + 1442695040888963407ull; *(LAS u32x2*)MISC = (u32x2){(unsigned)tag, (unsigned)(tag >> 32)}; }
      const float dt = rbf(__builtin_amdgcn_exp2f(ldtv * 1.442695041f));
      const int r31 = lane & 31;
      const u64 gt = (r31 & 16) ? ((r31 & 8) ? 0x001a191600181714ull : 0x15121110130f0e0dull) : ((r31 & 8) ? 0x0c0b0a0a07060504ull : 0x0a09080803020100ull);
      const unsigned ga = tb + (32 + (unsigned)((gt >> (8 * (r31 & 7))) & 31)) * 272 + (lane >> 5) * 16, ea = tb + r31 * 272 + (lane >> 5) * 16;
      const unsigned wb = tb + lane * 4;
      f32x16 c;
#pragma unroll
      for (int j = 0; j < 16; ++j) c[j] = 0.f;
#pragma unroll 1
      for (int ck = 0; ck < 2; ++ck) {
        const int sh = ck ? 0 : 16;
        const float zr2 = -rbf(__builtin_amdgcn_exp2f(bf_hi(p_lar << sh) * 1.442695041f)) * dt * 1.442695041f;
        const float zv = bf_hi(p_aim << sh) * dt * 0.1591549431f;
        const f32x2 w8 = cexp(zr2, zv, 8.f);
        const cw W8 = mkw(w8);
        {
          const cw W32 = mkw(cexp(zr2, zv, 32.f));
          f32x2 ev[4];
          ev[0] = (f32x2){1.f, 0.f}; ev[1] = w8; ev[2] = cmul(w8, W8); ev[3] = cmul(ev[2], W8);
#pragma unroll
          for (int i = 0; i < 32; ++i) {
            *(LAS unsigned*)(wb + i * 272) = pk(ev[i & 3].x, ev[i & 3].y);
            if (i < 28) ev[i & 3] = cmul(ev[i & 3], W32);
          }
        }
        STAMP(17 + 3 * ck);
        SBAR();
        {
          const f32x2 w1 = cexp(zr2, zv, 1.f);
          const cw W1 = mkw(w1), W2 = mkw(cmul(w1, W1)), W4 = mkw(cmul(W2.a, W2)), W256 = mkw(cexp(zr2, zv, 256.f));
          f32x2 g[9];
          g[0] = (f32x2){bf_hi(p_cre << sh), bf_hi(p_cim << sh)} * (2.f * dt);
          g[1] = cmul(g[0], W1); g[2] = cmul(g[0], W2); g[3] = cmul(g[1], W2);
#pragma unroll
          for (int i = 0; i < 4; ++i) g[4 + i] = cmul(g[i], W4);
          g[8] = cmul(g[0], W8);
          int row = 32;
#pragma unroll
          for (int i = 0; i < 8; ++i) *(LAS unsigned*)(wb + (row++) * 272) = pk(g[i].x, -g[i].y);
#pragma unroll
          for (int bk = 1; bk < 8; ++bk) {
            const int stp = bk == 1 ? 2 : (bk < 4 ? 4 : 8);
#pragma unroll
            for (int i = 0; i <= 8; i += stp) { g[i] = cmul(g[i], W256); *(LAS unsigned*)(wb + (row++) * 272) = pk(g[i].x, -g[i].y); }
          }
        }
        STAMP(18 + 3 * ck);
        BAR();
#pragma unroll
        for (int s = 0; s < 8; ++s) c = mfma32(lds128(ga + s * 32), lds128(ea + s * 32), c);
        STAMP(19 + 3 * ck);
        BAR();
      }
      unsigned pe3 = 0, po2 = 0;
      const unsigned tw = tb + 128 + lane * 64, sx = (((lane >> 1) + 1) & 3) << 4;
      if (lane < 8) *(LAS u32x4*)(tb + lane * 16) = (u32x4){0, 0, 0, 0};
#pragma unroll
      for (int st = 0; st < 4; ++st) {
        float k[8];
        if (st == 0) {
#pragma unroll
          for (int r = 0; r < 8; ++r) k[r] = c[r];
        } else if (st == 1) {
          k[0] = k[1] = k[2] = c[8]; k[3] = k[4] = k[5] = c[9]; k[6] = k[7] = c[10];
        } else {
          const float lo = c[7 + 2 * st], hi = c[8 + 2 * st];
          k[0] = k[1] = k[2] = k[3] = lo; k[4] = (lane & 1) ? hi : lo; k[5] = k[6] = k[7] = hi;
        }
        const unsigned e0 = pk(k[0], k[1]), e1 = pk(k[2], k[3]), e2 = pk(k[4], k[5]), e3 = pk(k[6], k[7]);
        const unsigned o0 = pk(k[1], k[2]), o1 = pk(k[3], k[4]), o2 = pk(k[5], k[6]);
        const unsigned pe = __builtin_amdgcn_update_dpp(pe3, e3, 0x138, 0xf, 0xf, false), po = __builtin_amdgcn_update_dpp(po2, o2, 0x138, 0xf, 0xf, false);
        pe3 = __builtin_amdgcn_readlane(e3, 63); po2 = __builtin_amdgcn_readlane(o2, 63);
        const unsigned om = __builtin_amdgcn_alignbit(e0, pe, 16);
        *(LAS u32x4*)(tw + st * 4096 + (0 ^ sx)) = (u32x4){e0, e1, om, o0};
        *(LAS u32x4*)(tw + st * 4096 + (16 ^ sx)) = (u32x4){pe, e0, po, om};
        *(LAS u32x4*)(tw + st * 4096 + (32 ^ sx)) = (u32x4){e2, e3, o1, o2};
        *(LAS u32x4*)(tw + st * 4096 + (48 ^ sx)) = (u32x4){e1, e2, o0, o1};
      }
      STAMP(1);
      BAR();
      poll((const GAS u64*)(ws + WS_UF) + ((b >> 3) & 3) * 16 + (lane & 15), ldtag());
      STAMP(2);
      u32x4 uu[4];
#pragma unroll
      for (int j = 0; j < 4; ++j) uu[j] = __builtin_amdgcn_raw_buffer_load_b128(wsr, lane * 16, WS_U + b * 8192 + ((w - 6) * 4 + j) * 1024, 16);
      if (w == 6) *(LAS u32x4*)(UB + lane * 16) = (u32x4){0, 0, 0, 0};
#pragma unroll
      for (int j = 0; j < 4; ++j) *(LAS u32x4*)(UB + 1024 + ((w - 6) * 4 + j) * 1024 + lane * 16) = uu[j];
    }
    BAR();
    STAMP(3);
    const int ch = w & 1, ts = (w - 4) >> 1, d = 2 * b + ch;
    const float dskip = bf_lo(dsk[d]);
    f32x4 acc[4];
#pragma unroll
    for (int t = 0; t < 4; ++t) acc[t] = (f32x4){0.f, 0.f, 0.f, 0.f};
    if (ts) conv_body<1>(TB0 + ch * TSZ, ch, lane, acc); else conv_body<0>(TB0 + ch * TSZ, ch, lane, acc);
    STAMP(4);
    {
      const int q = lane >> 4, c = lane & 15;
      const int o = 32 + 2 * c + (q >> 1);
      const unsigned ua = UB + o * 32 + ((ch ^ ((o >> 3) & 1)) << 4) + (q & 1) * 8;
      u32x2 pa[4];
#pragma unroll
      for (int t = 0; t < 4; ++t) {
        const int tt = 2 * t + ((t ^ ts) & 1);
        const u32x2 uv = lds64(ua + tt * 1024);
        pa[t] = (u32x2){pk(acc[t][0] + bf_lo(uv[0]) * dskip, acc[t][1] + bf_hi(uv[0]) * dskip), pk(acc[t][2] + bf_lo(uv[1]) * dskip, acc[t][3] + bf_hi(uv[1]) * dskip)};
      }
      const int tl = (q & 1) ? 3 - ts : ts;
      const unsigned av = (4 * tl + (c >> 2)) * 128 + ((4 * (q >> 1) + (c & 3)) << 4);
#pragma unroll
      for (int t = 0; t < 2; ++t) {
        const auto s0 = __builtin_amdgcn_permlane16_swap(pa[2 * t][0], pa[2 * t + 1][0], false, false);
        const auto s1 = __builtin_amdgcn_permlane16_swap(pa[2 * t][1], pa[2 * t + 1][1], false, false);
        st128((u32x4){s0[0], s1[0], s0[1], s1[1]}, av, rws, WS_A + d * 4096 + t * 2048);
      }
    }
    const u64 tag = ldtag();
    publish((GAS u64*)(ws + WS_AF) + ts * 512 + d, tag, lane);
    STAMP(5);
    BAR();
    const int a = w - 4;
#pragma unroll 1
    for (int j = 20; j < 28; ++j) dma(j * 4096 + a * 1024, wdv, rwd, wd_soff(7 + j, a));
    __builtin_amdgcn_s_barrier();
#pragma unroll 1
    for (int j = 28; j < 36; ++j) dma(j * 4096 + a * 1024, wdv, rwd, wd_soff(7 + j, a));
    WAITV(0);
    STAMP(6);
    __builtin_amdgcn_s_barrier();
    {
      const int t = m2 >> 2, tsn = (t ^ (t >> 1)) & 1;
      poll((const GAS u64*)(ws + WS_AF) + tsn * 512 + n2 * 64 + a * 16 + (lane & 15), tag);
      STAMP(7);
      u32x4 av[2];
#pragma unroll
      for (int j = 0; j < 2; ++j) av[j] = __builtin_amdgcn_raw_buffer_load_b128(wsr, (lane >> 3) * 4096 + (lane & 7) * 16, WS_A + (n2 * 64 + a * 16 + j * 8) * 4096 + m2 * 128, 16);
#pragma unroll
      for (int j = 0; j < 2; ++j) *(LAS u32x4*)(AT + (a * 2 + j) * 1024 + lane * 16) = av[j];
    }
    BAR();
    if (b == 0 && w == 4) {
#pragma unroll 1
      for (int j = 0; j < 4; ++j) poll((const GAS u64*)(ws + WS_AF) + 2 * (j * 64 + lane), tag);
      if (lane == 0) __hip_atomic_store((GAS u64*)ws, tag, RLX);
    }
  }
  STAMP_END();
}

extern "C" void kernel_launch(void* const* d_in, const int* in_sizes, int n_in, void* d_out, int out_size, void* d_ws, size_t ws_size,
                              hipStream_t stream) {
  (void)hipFuncSetAttribute((const void*)fused_kernel, hipFuncAttributeMaxDynamicSharedMemorySize, LDS_BYTES);
  fused_kernel<<<256, 512, LDS_BYTES, stream>>>((const char*)d_in[0], (const unsigned*)d_in[1], (const unsigned*)d_in[2], (const unsigned*)d_in[3],
                                                (const unsigned*)d_in[4], (const unsigned short*)d_in[5], (const unsigned short*)d_in[6],
                                                (const char*)d_in[8], (const char*)d_in[9], (const char*)d_in[10], (const char*)d_in[11],
                                                (char*)d_out, (char*)d_ws);
}
```

```cpp
#include <hip/hip_runtime.h>
#include <stdint.h>
#pragma clang diagnostic ignored "-Wint-to-pointer-cast"

typedef unsigned long long u64;
typedef __bf16 bf16x8 __attribute__((ext_vector_type(8)));
typedef __bf16 bf16x2 __attribute__((ext_vector_type(2)));
typedef short s16x4 __attribute__((ext_vector_type(4)));
typedef short s16x8 __attribute__((ext_vector_type(8)));
typedef float f32x2 __attribute__((ext_vector_type(2)));
typedef float f32x4 __attribute__((ext_vector_type(4)));
typedef float f32x16 __attribute__((ext_vector_type(16)));
typedef unsigned u32x2 __attribute__((ext_vector_type(2)));
typedef unsigned u32x4 __attribute__((ext_vector_type(4)));
#define LAS __attribute__((address_space(3)))
#define GAS __attribute__((address_space(1)))
#define DI __device__ __forceinline__

constexpr int L_ = 2048, D_ = 512, F_ = 1376;
constexpr unsigned SLOT = 40992, WOFF = 16384, WUP = 12320;
constexpr unsigned WLIN = 64, UB = 3 * SLOT - 9216;
constexpr unsigned TB0 = 3 * SLOT, TSZ = 16512;
constexpr unsigned AT = TB0, XCH = TB0 + 8192, STG = XCH + 16384;
constexpr unsigned MISC = TB0 + 2 * TSZ;
constexpr unsigned F_LAND = MISC + 16, F_KD = MISC + 24, F_G = MISC + 32, F_H = MISC + 48, KX = MISC + 64, GWL = SLOT + WOFF;
constexpr unsigned LDS_BYTES = 163840;
constexpr unsigned WS_G = 4096, WS_UF = 16384, WS_HF = WS_UF + 65536;
constexpr unsigned WS_U = 524288, WS_A = WS_U + 2097152, WS_H = WS_A + 4194304, WS_END = WS_H + 64 * 88064;
constexpr unsigned WS_ST = 16u << 20;
constexpr unsigned CODE_LINES = 152;
#define ROT 11
#define ROT2 18

#define STAMP_INIT()
#define STAMP(i)
#define STAMP_END()

DI float bf_lo(unsigned v) { return __uint_as_float(v << 16); }
DI float bf_hi(unsigned v) { return __uint_as_float(v & 0xffff0000u); }
DI unsigned pk(float a, float b) { f32x2 t = {a, b}; return __builtin_bit_cast(unsigned, __builtin_convertvector(t, bf16x2)); }
DI float rbf(float a) { return bf_lo(pk(a, a)); }
DI u32x4 lds128(unsigned a) { return *(LAS u32x4*)a; }
DI u32x2 lds64(unsigned a) { return *(LAS u32x2*)a; }
DI u32x2 ldtr(unsigned a) { return __builtin_bit_cast(u32x2, __builtin_amdgcn_ds_read_tr16_b64_v4i16((LAS s16x4*)a)); }
DI u32x4 ldtr2(unsigned a, unsigned b) { u32x2 x = ldtr(a), y = ldtr(b); return (u32x4){x[0], x[1], y[0], y[1]}; }
DI f32x16 mfma32(u32x4 a, u32x4 b, f32x16 c) { return __builtin_amdgcn_mfma_f32_32x32x16_bf16(__builtin_bit_cast(bf16x8, a), __builtin_bit_cast(bf16x8, b), c, 0, 0, 0); }
DI f32x4 mfma16(u32x4 a, u32x4 b, f32x4 c) { return __builtin_amdgcn_mfma_f32_16x16x32_bf16(__builtin_bit_cast(bf16x8, a), __builtin_bit_cast(bf16x8, b), c, 0, 0, 0); }
DI void mfma16a(f32x4& c, u32x4 a, u32x4 b) { asm volatile("v_mfma_f32_16x16x32_bf16 %0, %1, %2, %0" : "+v"(c) : "v"(a), "v"(b)); }
DI u32x4 mkrsrc(const void* p, unsigned bytes) { u64 a = (u64)p; return (u32x4){(unsigned)a, (unsigned)(a >> 32) & 0xffffu, bytes, 0x00020000u}; }
DI void dma(unsigned dst, unsigned voff, u32x4 rs, unsigned soff) {
  asm volatile("s_mov_b32 m0, %0\n\ts_nop 0\n\tbuffer_load_dwordx4 %1, %2, %3 offen lds" ::"s"(dst), "v"(voff), "s"(rs), "s"(soff) : "memory");
}
DI void dma4(unsigned dst, unsigned v0, unsigned v1, unsigned v2, unsigned v3, u32x4 rs, unsigned soff) {
  asm volatile("s_mov_b32 m0, %0\n\ts_nop 0\n\tbuffer_load_dwordx4 %1, %5, %6 offen lds\n\tbuffer_load_dwordx4 %2, %5, %6 offen offset:1024 lds\n\t"
               "buffer_load_dwordx4 %3, %5, %6 offen offset:2048 lds\n\tbuffer_load_dwordx4 %4, %5, %6 offen offset:3072 lds"
               ::"s"(dst), "v"(v0), "v"(v1), "v"(v2), "v"(v3), "s"(rs), "s"(soff) : "memory");
}
DI void st128(u32x4 v, unsigned voff, u32x4 rs, unsigned soff) {
  asm volatile("buffer_store_dwordx4 %0, %1, %2, %3 offen sc1\n\ts_nop 1" ::"v"(v), "v"(voff), "s"(rs), "s"(soff) : "memory");
}
#define WAITV(n) asm volatile("s_waitcnt vmcnt(" #n ")" ::: "memory")
#define WAITL() asm volatile("s_waitcnt lgkmcnt(0)" ::: "memory")
#define FENCE() asm volatile("" ::: "memory")
#define OPQ(x) asm volatile("" : "+v"(x))
#define SB() __builtin_amdgcn_sched_barrier(0)
#define BAR() do { asm volatile("s_waitcnt lgkmcnt(0)" ::: "memory"); __builtin_amdgcn_s_barrier(); asm volatile("" ::: "memory"); } while (0)
#define RLX __ATOMIC_RELAXED, __HIP_MEMORY_SCOPE_AGENT
constexpr unsigned SPIN = 1u << 16;
DI GAS u64* flag(char* ws, unsigned base, unsigned i) { return (GAS u64*)(ws + (base + i * 128)); }
DI void poll(const GAS u64* p, u64 tag) {
#pragma unroll 1
  for (unsigned spins = 0; spins < SPIN; ++spins) {
    u64 v = __hip_atomic_load(p, RLX);
    if (__all(v == tag)) break;
    __builtin_amdgcn_s_sleep(2);
  }
  FENCE();
}
DI void publish(GAS u64* p, u64 tag, int lane) {
  WAITV(0);
  if (lane == 0) __hip_atomic_store(p, tag, RLX);
}
DI void lset(unsigned a, unsigned v) { FENCE(); *(volatile LAS unsigned*)a = v; FENCE(); }
DI unsigned lget(unsigned a) { return *(volatile LAS unsigned*)a; }
DI void lwait(unsigned a, unsigned v) {
  unsigned t, n;
  asm volatile("s_mov_b32 %1, 0x400000\n"
               "1:\n\tds_read_b32 %0, %2\n\ts_waitcnt lgkmcnt(0)\n\tv_cmp_gt_u32 vcc, %3, %0\n\ts_and_b64 vcc, exec, vcc\n\ts_cbranch_vccz 2f\n\t"
               "s_sleep 1\n\ts_sub_u32 %1, %1, 1\n\ts_cbranch_scc0 1b\n2:"
               : "=&v"(t), "=&s"(n) : "v"(a), "v"(v) : "vcc", "scc", "memory");
}
DI u64 ldtag() { u32x2 t = lds64(MISC); return ((u64)(unsigned)__builtin_amdgcn_readfirstlane(t[1]) << 32) | (unsigned)__builtin_amdgcn_readfirstlane(t[0]); }

DI float add1(float a, float b) { float r; asm("v_add_f32 %0, %1, %2" : "=v"(r) : "v"(a), "v"(b)); return r; }
struct cw { f32x2 a, b; };
DI cw mkw(f32x2 w) { return cw{w, (f32x2){-w.y, w.x}}; }
DI f32x2 cmul(f32x2 v, cw w) { return __builtin_elementwise_fma(v.yy, w.b, v.xx * w.a); }
DI f32x2 cexp(float zr2, float zv, float p) {
  const float m = __builtin_amdgcn_exp2f(zr2 * p), a = __builtin_amdgcn_fractf(zv * p);
  return (f32x2){__builtin_amdgcn_cosf(a), __builtin_amdgcn_sinf(a)} * m;
}

template <int TS>
DI void conv_body(unsigned tb, int ch, int lane, f32x4 (&acc)[4]) {
  constexpr int T0 = TS ? 1 : 0, T1 = TS ? 2 : 3, T2 = TS ? 5 : 4, T3 = TS ? 6 : 7;
  const int q = lane >> 4, e = (lane >> 2) & 3, pp = lane & 3, c = lane & 15;
  unsigned a0[2], a1[2];
#pragma unroll
  for (int r = 0; r < 2; ++r) {
    const int y0 = 8 * r + 8 - 2 * q + pp, y1 = y0 - 1;
    a0[r] = tb + (y0 >> 1) * 64 + (((((y0 & 1) << 1) | (e >> 1)) ^ ((y0 >> 2) & 3)) << 4) + (e & 1) * 8;
    a1[r] = tb + (y1 >> 1) * 64 + (((((y1 & 1) << 1) | (e >> 1)) ^ ((y1 >> 2) & 3)) << 4) + (e & 1) * 8;
  }
  int o = 30 + 2 * c + q;
  auto baddr = [&]() { const unsigned a = UB + o * 32 + ((ch ^ ((o >> 3) & 1)) << 4); o -= 4; return a; };
  u32x4 A[2][T3 + 1], B[2][T3 + 1];
  unsigned ab = baddr();
  OPQ(ab); OPQ(a0[0]); OPQ(a1[0]);
#pragma unroll
  for (int p = 0; p <= T3; ++p) { A[0][p] = ldtr2(a0[0] + p * 2048, a1[0] + p * 2048); B[0][p] = lds128(ab + p * 1024); }
  lwait(F_G + (lane & 3) * 4, 10);
#pragma unroll 1
  for (int it = 0; it < 4; ++it) {
#pragma unroll
    for (int hf = 0; hf < 2; ++hf) {
      if (hf) { a0[0] += 512; a1[0] += 512; }
      ab = baddr();
      OPQ(ab); OPQ(a0[hf ^ 1]); OPQ(a1[hf ^ 1]);
      int ri = 0;
      auto rd = [&]() {
        if (ri <= 2 * T3 + 1) {
          const int p = ri >> 1;
          if (ri & 1) A[hf ^ 1][p] = ldtr2(a0[hf ^ 1] + p * 2048, a1[hf ^ 1] + p * 2048);
          else B[hf ^ 1][T3 - p] = lds128(ab + (T3 - p) * 1024);
        }
        ++ri;
        SB();
      };
      SB();
#pragma unroll
      for (int p = 0; p <= T3; ++p) {
        mfma16a(acc[3], A[hf][p], B[hf][T3 - p]); SB(); rd();
        if (p <= T2) { mfma16a(acc[2], A[hf][p], B[hf][T2 - p < 0 ? 0 : T2 - p]); SB(); rd(); }
        if (p >= T3 - T1) { mfma16a(acc[1], A[hf][T3 - p], B[hf][T1 - T3 + p < 0 ? 0 : T1 - T3 + p]); SB(); rd(); }
        if (p >= T3 - T0) { mfma16a(acc[0], A[hf][T3 - p], B[hf][T0 - T3 + p < 0 ? 0 : T0 - T3 + p]); SB(); rd(); }
      }
      if (!hf) { a0[1] += 512; a1[1] += 512; }
    }
  }
  asm volatile("s_nop 7" : "+v"(acc[0]), "+v"(acc[1]), "+v"(acc[2]), "+v"(acc[3]));
}

typedef unsigned u32x8 __attribute__((ext_vector_type(8)));
typedef unsigned u32x16 __attribute__((ext_vector_type(16)));
DI u64 mk64(unsigned lo, unsigned hi) { return ((u64)hi << 32) | lo; }
struct Args { const void* p[13]; };
#define ARGS_READY() asm volatile("s_waitcnt lgkmcnt(0)" : "+s"(ka), "+s"(kb), "+s"(kc)::"memory"); \
  char* const ws = (char*)mk64(kc[0], kc[1]); \
  const __amdgpu_buffer_rsrc_t wsr = __builtin_amdgcn_make_buffer_rsrc(ws, 0, WS_END, 0x00020000); \
  const u32x4 rwd = {ka[6], ka[7], F_ * D_ * 2, 0x00020000u}, rws = {kc[0], kc[1], WS_END, 0x00020000u};

__global__ __launch_bounds__(512, 2) void fused_kernel(Args) {
  u32x16 ka; u32x8 kb; u32x2 kc;
  asm volatile("s_load_dwordx16 %0, %3, 0x0\n\ts_load_dwordx8 %1, %3, 0x40\n\ts_load_dwordx2 %2, %3, 0x60"
               : "=&s"(ka), "=&s"(kb), "=&s"(kc) : "s"(__builtin_amdgcn_kernarg_segment_ptr()) : "memory");
  SB();
  const int b = blockIdx.x, xcd = b & 7, bi = b >> 3;
  const int lane = threadIdx.x & 63, w = __builtin_amdgcn_readfirstlane(threadIdx.x >> 6);
  unsigned cpf = 0;
  if (w == 2) {
    u64 pc; asm volatile("s_getpc_b64 %0" : "=s"(pc));
    cpf = *(const GAS unsigned*)((pc & ~127ull) + (unsigned)min(bi * 8 + (lane & 7), (int)CODE_LINES - 1) * 128);
    *(LAS unsigned*)(F_LAND + min(lane, 11) * 4) = 0;
  }
  const int rt = (xcd & 1) * 8 + (bi & 7), ct = (xcd >> 1) * 4 + (bi >> 3);
  const int m2 = xcd * 4 + (bi & 3), n2 = bi >> 2;
  const int k0 = (bi + (bi >> 3) * 2) & 7;
  const bool spare = ct == 15;
  const unsigned l1 = (lane & 1) * 4, l3 = (lane & 3) * 4;
  const unsigned wdv = (lane >> 3) * 1024 + (((lane & 7) ^ ((lane >> 2) & 4)) << 4);
  const int rot0 = (m2 & 3) * ROT + (n2 & 1) * ROT2, rot = rot0 >= 43 ? rot0 - 43 : rot0;
  auto kstep = [&](int j) { const int ks = j + rot; return ks >= 43 ? ks - 43 : ks; };
#define WD_STEP(j, idx) dma4(WLIN + (idx) * 4096, wdv, wdv + 7168, wdv + 14336, wdv + 21504, rwd, kstep(j) * 32768 + n2 * 128)
  unsigned vx[8], vw[12];
#pragma unroll
  for (int a = 0; a < 8; ++a) { vx[a] = (lane >> 3) * 1024 + (((lane & 7) ^ ((4 * a + (lane >> 4)) & 7)) << 4) + a * 8192 - (a & 3) * 1024; OPQ(vx[a]); }
#pragma unroll
  for (int c = 0; c < 12; ++c) {
    const unsigned ee = 64 * (c % 3) + lane;
    vw[c] = (ee / 12) * 2752 + ((ee % 12) & (ct == 14 ? 3 : 15)) * 16 + (c / 3) * 44032 - (c & 3) * 1024; OPQ(vw[c]);
  }
  unsigned po = ((2 * b + (w & 1)) * 64 + lane) * 4; OPQ(po);
  ARGS_READY();
  STAMP_INIT(); STAMP(0);
  const bool kgen = ((w + 2) & 7) < 4;
  unsigned p_lar = 0, p_aim = 0, p_cre = 0, p_cim = 0, p_ldt = 0;
  u64 ep = 0;
  if (kgen) {
    p_lar = *(const GAS unsigned*)(mk64(ka[8], ka[9]) + po); p_aim = *(const GAS unsigned*)(mk64(ka[10], ka[11]) + po);
    p_ldt = ((const GAS unsigned*)mk64(kb[0], kb[1]))[b];
    p_cre = *(const GAS unsigned*)(mk64(ka[12], ka[13]) + po); p_cim = *(const GAS unsigned*)(mk64(ka[14], ka[15]) + po);
    ep = __hip_atomic_load((const GAS u64*)ws, RLX);
  }

  if (__builtin_expect((w >> 1) == 2, 1)) {
      const int j = w - 4;
      const u32x4 rx = {ka[0], ka[1], L_ * D_ * 2, 0x00020000u}, rw = {j ? ka[4] : ka[2], j ? ka[5] : ka[3], D_ * F_ * 2, 0x00020000u};
      if (spare && j == 0) dma(GWL, lane * 16, (u32x4){kb[4], kb[5], 1024, 0x00020000u}, 0);
      unsigned sb = 0;
#pragma unroll 1
      for (int k = 0; k < 8; ++k) {
        const int kt = (k + k0) & 7;
        const unsigned sx = (rt * 128 + 64 * j) * 1024 + kt * 128, sw = kt * 64 * 2752 + ct * 192;
#pragma unroll
        for (int a = 0; a < 8; a += 4) dma4(sb + (8 * j + a) * 1024, vx[a], vx[a + 1], vx[a + 2], vx[a + 3], rx, sx);
        if (!spare) {
#pragma unroll
          for (int c = 0; c < 12; c += 4) dma4(sb + WOFF + j * WUP + c * 1024, vw[c], vw[c + 1], vw[c + 2], vw[c + 3], rw, sw);
          WAITV(20);
        } else WAITV(8);
#define PF(j) "s_load_dword s90, %0, " #j "*32768\n\t"
        if (k >= 2) asm volatile(PF(0) PF(1) PF(2) PF(3) PF(4) PF(5) PF(6) ::"s"(mk64(ka[6], ka[7]) + (2 * b + j) * 64 + (k - 2) * 7 * 32768) : "s90", "memory");
        if (k < 2) STAMP(15 + k);
        if (k) __builtin_amdgcn_s_barrier();
        sb = sb == 2 * SLOT ? 0 : sb + SLOT;
      }
      asm volatile(PF(0) ::"s"(mk64(ka[6], ka[7]) + (2 * b + j) * 64 + 42 * 32768) : "s90", "memory");
#pragma unroll 1
      for (int p = j; p < 8; p += 2) WD_STEP(min(p, 6), 20 + min(p, 6));
      WAITV(16);
      __builtin_amdgcn_s_barrier();
#pragma unroll 1
      for (int p = j; p < 9; p += 2) { WAITV(10); WD_STEP(7 + p, p); }
      STAMP(1);
      WAITV(0);
      lset(F_LAND + 4 * j, 9);
      STAMP(2);
  } else {
    f32x16 c;
    asm volatile("" : "=v"(c));
    f32x16 acc1[3][2];
    {
      u32x4 z = {0, 0, 0, 0}; OPQ(z);
#pragma unroll
      for (int g = 0; g < 6; ++g) asm volatile("s_nop 1\n\tv_mfma_f32_32x32x16_bf16 %0, %1, %1, 0" : "=&v"(acc1[g >> 1][g & 1]) : "v"(z));
    }
    const int h = lane >> 5, l31 = lane & 31, mh = w & 1, nh = w >> 1;
      unsigned xo[4];
#pragma unroll
      for (int s = 0; s < 4; ++s) { xo[s] = (64 * mh + l31) * 128 + (((2 * s + h) ^ ((l31 >> 1) & 7)) << 4); OPQ(xo[s]); }
      unsigned wo = WOFF + ((lane >> 4) & 1) * WUP + (8 * h + ((lane >> 2) & 3)) * 192 + nh * 96 + (lane & 1) * 16 + (lane & 2) * 4;
      OPQ(wo);
      unsigned hso[3];
#pragma unroll
      for (int g = 0; g < 3; ++g) {
        const int g16 = ct * 6 + nh * 3 + g, p0 = g16 - ((2 * rt + mh) & 3) * (2 * ROT);
        hso[g] = ((g16 < 86 ? (rt * 4 + 2 * mh) * 86 : 64 * 86) + (p0 < 0 ? p0 + 86 : p0)) * 1024 + WS_H;
        asm volatile("" : "+s"(hso[g]));
      }
    const unsigned tb = TB0 + (w & 1) * TSZ;
    if (kgen) {
      const unsigned kb0 = w < 2 ? 2 * SLOT + w * TSZ : tb;
      const int r31 = lane & 31, sh = w < 2 ? 0 : 16;
      const u64 gt = (r31 & 16) ? ((r31 & 8) ? 0x001a191600181714ull : 0x15121110130f0e0dull) : ((r31 & 8) ? 0x0c0b0a0a07060504ull : 0x0a09080803020100ull);
      unsigned ga = kb0 + (32 + (unsigned)((gt >> (8 * (r31 & 7))) & 31)) * 272 + (lane >> 5) * 16, ea = kb0 + r31 * 272 + (lane >> 5) * 16;
      unsigned wb = kb0 + lane * 4;
      OPQ(ga); OPQ(ea); OPQ(wb);
      const float dt = rbf(__builtin_amdgcn_exp2f(bf_hi(p_ldt << ((w & 1) ? 0 : 16)) * 1.442695041f));
      const float zr2 = -rbf(__builtin_amdgcn_exp2f(bf_hi(p_lar << sh) * 1.442695041f)) * dt * 1.442695041f;
      const float zv = bf_hi(p_aim << sh) * dt * 0.1591549431f;
      STAMP(16);
      {
        const cw w8 = mkw(cexp(zr2, zv, 8.f)), w32 = mkw(cexp(zr2, zv, 32.f));
        f32x2 ev[4];
        ev[0] = (f32x2){1.f, 0.f}; ev[1] = w8.a; ev[2] = cmul(w8.a, w8); ev[3] = cmul(ev[2], w8);
#pragma unroll
        for (int i = 0; i < 32; ++i) {
          *(LAS unsigned*)(wb + i * 272) = pk(ev[i & 3].x, ev[i & 3].y);
          if (i < 28) ev[i & 3] = i ? cmul(ev[i & 3], w32) : w32.a;
        }
      }
      u32x4 fa[8], fb[8];
      FENCE();
#pragma unroll
      for (int s = 0; s < 8; ++s) fb[s] = lds128(ea + s * 32);
      FENCE();
      {
        const cw w1 = mkw(cexp(zr2, -zv, 1.f)), w2 = mkw(cmul(w1.a, w1)), w4 = mkw(cmul(w2.a, w2)), w256 = mkw(cexp(zr2, -zv, 256.f));
        f32x2 g[9];
        g[0] = (f32x2){bf_hi(p_cre << sh), -bf_hi(p_cim << sh)} * (2.f * dt);
        g[1] = cmul(g[0], w1); g[2] = cmul(g[0], w2); g[3] = cmul(g[1], w2);
#pragma unroll
        for (int i = 0; i < 5; ++i) g[4 + i] = cmul(g[i], w4);
        int row = 32;
#pragma unroll
        for (int i = 0; i < 8; ++i) *(LAS unsigned*)(wb + (row++) * 272) = pk(g[i].x, g[i].y);
#pragma unroll
        for (int bk = 1; bk < 8; ++bk) {
          const int stp = bk == 1 ? 2 : (bk < 4 ? 4 : 8);
#pragma unroll
          for (int i = 0; i <= 8; i += stp) { g[i] = cmul(g[i], w256); *(LAS unsigned*)(wb + (row++) * 272) = pk(g[i].x, g[i].y); }
        }
      }
      FENCE();
      STAMP(18);
#pragma unroll
      for (int s = 0; s < 8; ++s) fa[s] = lds128(ga + s * 32);
      SB();
#pragma unroll
      for (int j = 0; j < 16; ++j) c[j] = 0.f;
#pragma unroll
      for (int s = 0; s < 8; ++s) c = mfma32(fa[s], fb[s], c);
      FENCE();
    }
    const unsigned kx = KX + (w & 1) * 3840;
    if (w < 4) {
    if (w < 2) {
#pragma unroll
      for (int j = 0; j < 3; ++j) *(LAS f32x4*)(kx + j * 1024 + lane * 16) = (f32x4){c[4 * j], c[4 * j + 1], c[4 * j + 2], c[4 * j + 3]};
#pragma unroll
      for (int j = 0; j < 3; ++j) *(LAS float*)(kx + 3072 + j * 256 + lane * 4) = c[12 + j];
    }
    asm volatile("s_waitcnt lgkmcnt(0)" ::"v"(ep), "v"(cpf) : "memory");
    STAMP(19);
    if (!spare) {
      f32x16 (&acc)[3][2] = acc1;
      u32x4 xb[2][2], wa[2][3];
      __builtin_amdgcn_s_barrier(); FENCE();
      STAMP(1);
      xb[0][0] = lds128(xo[0]); wa[0][0] = ldtr2(wo, wo + 768); xb[0][1] = lds128(xo[0] + 4096);
      wa[0][1] = ldtr2(wo + 32, wo + 800); wa[0][2] = ldtr2(wo + 64, wo + 832);
      unsigned sb = 0;
#pragma unroll 1
      for (int k = 0; k < 8; ++k) {
#pragma unroll
        for (int s = 0; s < 4; ++s) {
          const int c = s & 1, n = c ^ 1, sn = (s + 1) & 3;
          auto r0 = [&]() { xb[n][0] = lds128(xo[sn]); wa[n][0] = ldtr2(wo + sn * 3072, wo + sn * 3072 + 768); SB(); };
          auto r1 = [&]() { xb[n][1] = lds128(xo[sn] + 4096); SB(); };
          auto r2 = [&]() { wa[n][1] = ldtr2(wo + sn * 3072 + 32, wo + sn * 3072 + 800); SB(); };
          auto r3 = [&]() { wa[n][2] = ldtr2(wo + sn * 3072 + 64, wo + sn * 3072 + 832); SB(); };
          SB();
          acc[0][0] = mfma32(wa[c][0], xb[c][0], acc[0][0]); SB();
          if (s < 3) r0();
          else {
            const unsigned dl = sb == 2 * SLOT ? -2 * SLOT : SLOT;
            sb += dl; wo += dl;
#pragma unroll
            for (int i = 0; i < 4; ++i) { xo[i] += dl; OPQ(xo[i]); }
            OPQ(wo); SB();
          }
          acc[0][1] = mfma32(wa[c][0], xb[c][1], acc[0][1]); SB();
          if (s < 3) r1();
          else {
            if (k < 7) BAR();
            SB(); r0();
          }
          acc[1][0] = mfma32(wa[c][1], xb[c][0], acc[1][0]); SB();
          if (s < 3) r2(); else r1();
          acc[1][1] = mfma32(wa[c][1], xb[c][1], acc[1][1]); SB();
          if (s < 3) r3(); else r2();
          acc[2][0] = mfma32(wa[c][2], xb[c][0], acc[2][0]); SB();
          if (s == 3) r3();
          acc[2][1] = mfma32(wa[c][2], xb[c][1], acc[2][1]); SB();
        }
      }
      STAMP(2);
#pragma unroll
      for (int g = 0; g < 3; ++g) {
#pragma unroll
        for (int t = 0; t < 2; ++t) {
          u32x4 o;
#pragma unroll
          for (int j = 0; j < 4; ++j) {
            const f32x2 gg = {acc[g][t][2 * j], acc[g][t][2 * j + 1]}, uu = {acc[g][t][8 + 2 * j], acc[g][t][9 + 2 * j]};
            const f32x2 z = gg * __builtin_elementwise_fma(gg * gg, (f32x2){-0.1029432397f, -0.1029432397f}, (f32x2){-2.302208198f, -2.302208198f});
            const f32x2 dn = (f32x2){__builtin_amdgcn_exp2f(z.x), __builtin_amdgcn_exp2f(z.y)} + 1.f;
            const f32x2 hh = gg * uu * (f32x2){__builtin_amdgcn_rcpf(dn.x), __builtin_amdgcn_rcpf(dn.y)};
            o[j] = pk(hh.x, hh.y);
          }
          st128(o, lane * 16 + t * 88064, rws, hso[g]);
        }
      }
    } else {
      f32x16 ga;
#pragma unroll
      for (int j = 0; j < 16; ++j) ga[j] = 0.f;
      unsigned sb = 0;
#pragma unroll 1
      for (int k = 0; k < 8; ++k) {
        BAR();
#pragma unroll
        for (int s = 0; s < 4; ++s)
          ga = mfma32(lds128(GWL + ((k + k0) & 7) * 128 + s * 32 + h * 16), lds128(sb + (32 * w + l31) * 128 + (((2 * s + h) ^ ((l31 >> 1) & 7)) << 4)), ga);
        sb = sb == 2 * SLOT ? 0 : sb + SLOT;
      }
      const float gate = __builtin_amdgcn_rcpf(1.f + __builtin_amdgcn_exp2f(ga[0] * -1.442695041f));
      if (lane < 32) __hip_atomic_store((GAS float*)(ws + WS_G) + rt * 128 + 32 * w + lane, gate, RLX);
    }
    STAMP(3);
    const int e = (lane >> 2) & 3, pp = lane & 3, blk = (lane >> 4) & 1;
    unsigned wa[2];
#pragma unroll
    for (int dt = 0; dt < 2; ++dt) wa[dt] = WLIN + (16 * nh + 8 * h + e) * 128 + ((dt ^ (e >> 1)) << 6) + (pp & 1) * 32 + blk * 16 + (pp >> 1) * 8;
    lwait(F_LAND + l1, 9);
    STAMP(5);
    u32x4 pre[16][2];
    unsigned wh[2] = {wa[0] + 20 * 4096, wa[1] + 20 * 4096};
    OPQ(wa[0]); OPQ(wa[1]); OPQ(wh[0]); OPQ(wh[1]);
#pragma unroll
    for (int j = 0; j < 16; ++j)
#pragma unroll
      for (int dt = 0; dt < 2; ++dt) { const unsigned a = j < 7 ? wh[dt] + j * 4096 : wa[dt] + (j - 7) * 4096; pre[j][dt] = ldtr2(a, a + 512); }
    lset(F_G + 4 * w, 10);
    const u64 tag = ldtag();
    lwait(F_G + l3, 10);
#pragma unroll 1
    for (int p = w; p < 27; p += 4) {
      WD_STEP(16 + p, p);
      if (p >> 2 == 2) {
        WAITV(12);
        if (lane == 0) __hip_atomic_store(flag(ws, WS_HF, spare ? (2 * rt + nh) * 32 + 30 + mh : (2 * rt + mh) * 32 + 2 * ct + nh), tag, RLX);
        STAMP(4);
      }
    }
    STAMP(6);
    unsigned hb = WS_H + (m2 * 2 + mh) * 88064 + nh * 1024 + (n2 & 1) * ROT2 * 2048, gso = WS_G + (m2 * 64 + mh * 32) * 4;
    asm volatile("" : "+s"(hb), "+s"(gso));
    if (w == 0) poll(flag(ws, WS_HF, m2 * 32 + l31), tag);
    WAITV(0);
    lset(F_G + 4 * w, 11);
    lwait(F_G + l3, 11);
    STAMP(7);

    auto ldh = [&](int j) { return __builtin_amdgcn_raw_buffer_load_b128(wsr, lane * 16 + (j & 1) * 2048, hb + (j >> 1) * 4096, 16); };
    u32x4 hr[9];
#pragma unroll
    for (int j = 0; j < 9; ++j) hr[(j + 2) % 9] = ldh(j);
    const float gate = __uint_as_float(__builtin_amdgcn_raw_buffer_load_b32(wsr, l31 * 4, gso, 16));
    f32x16 acc[2];
#pragma unroll
    for (int j = 0; j < 16; ++j) acc[0][j] = acc[1][j] = 0.f;
    u32x4 wf[3][2];
#pragma unroll
    for (int j = 0; j < 16; ++j) {
      SB();
      acc[0] = mfma32(pre[j][0], hr[(j + 2) % 9], acc[0]);
      acc[1] = mfma32(pre[j][1], hr[(j + 2) % 9], acc[1]);
      SB();
      hr[(j + 2) % 9] = ldh(j + 9);
      if (j >= 14) { wf[j - 14][0] = ldtr2(wa[0] + (j - 14) * 4096, wa[0] + (j - 14) * 4096 + 512); wf[j - 14][1] = ldtr2(wa[1] + (j - 14) * 4096, wa[1] + (j - 14) * 4096 + 512); }
    }
    hb += (n2 & 1) * ROT2 ? -18 * 2048 : 25 * 2048;
    STAMP(13);
#pragma unroll 1
    for (int it = 0; it < 3; ++it) {
#pragma unroll
      for (int u = 0; u < 9; ++u) {
        if (u == 7) {
          wa[0] += 9 * 4096; wa[1] += 9 * 4096; OPQ(wa[0]); OPQ(wa[1]);
        }
        const int un = (u + 2) % 9;
        SB();
        acc[0] = mfma32(wf[u % 3][0], hr[u], acc[0]); SB();
        wf[un % 3][0] = ldtr2(wa[0] + un * 4096, wa[0] + un * 4096 + 512); SB();
        acc[1] = mfma32(wf[u % 3][1], hr[u], acc[1]); SB();
        wf[un % 3][1] = ldtr2(wa[1] + un * 4096, wa[1] + un * 4096 + 512);
        hr[u] = ldh(u);
      }
      hb = it == 0 ? hb + 9 * 2048 : WS_END;
      lset(F_G + 4 * w, 12);
    }
    STAMP(8);
    lwait(F_H + l3, 1);
    const f32x16 snd = nh ? acc[0] : acc[1], keep = nh ? acc[1] : acc[0];
#pragma unroll
    for (int j = 0; j < 4; ++j) *(LAS f32x4*)(XCH + w * 4096 + j * 1024 + lane * 16) = (f32x4){snd[4 * j], snd[4 * j + 1], snd[4 * j + 2], snd[4 * j + 3]};
    BAR();
    STAMP(9);
    u32x4 o[2];
#pragma unroll
    for (int j = 0; j < 4; ++j) {
      const f32x4 r = *(LAS f32x4*)(XCH + (w ^ 2) * 4096 + j * 1024 + lane * 16);
      const u32x2 av = ldtr(AT + (32 * nh + 16 * h + 4 * j + e) * 128 + (8 * mh + 4 * blk + pp) * 8);
      const float b0 = keep[4 * j] + r[0], b1 = keep[4 * j + 1] + r[1], b2 = keep[4 * j + 2] + r[2], b3 = keep[4 * j + 3] + r[3];
      o[j >> 1][2 * (j & 1)] = pk(b0 + gate * (bf_lo(av[0]) - b0), b1 + gate * (bf_hi(av[0]) - b1));
      o[j >> 1][2 * (j & 1) + 1] = pk(b2 + gate * (bf_lo(av[1]) - b2), b3 + gate * (bf_hi(av[1]) - b3));
    }
    const unsigned st = STG + w * 2048;
#pragma unroll
    for (int t = 0; t < 2; ++t) *(LAS u32x4*)(st + l31 * 64 + (((2 * h + t) ^ ((l31 >> 1) & 3)) << 4)) = o[t];
    FENCE();
    const u32x4 rout = {kb[6], kb[7], L_ * D_ * 2, 0x00020000u};
#pragma unroll
    for (int t = 0; t < 2; ++t) {
      const int row = 16 * t + (lane >> 2);
      st128(lds128(st + row * 64 + (((lane & 3) ^ ((row >> 1) & 3)) << 4)), row * 1024 + (lane & 3) * 16, rout, (m2 * 64 + mh * 32) * 1024 + n2 * 128 + nh * 64);
    }
    STAMP(10);
    STAMP_END();
    __builtin_amdgcn_endpgm();
    }
      OPQ(ep);
      const u64 tag0 = ep * 6364136223846793005ull + 1442695040888963407ull;
      *(LAS u32x2*)MISC = (u32x2){(unsigned)tag0, (unsigned)(tag0 >> 32)};
      STAMP(19);
      const int late = xcd >> 2, c4 = (xcd & 2) + (w & 1), c16 = ((k0 + late) & 7) * 4 + c4;
      unsigned pe3 = 0, po2 = 0;
      unsigned tw = tb + 128 + lane * 64;
      const unsigned sx = (((lane >> 1) + 1) & 3) << 4;
      u32x4 T[4][4];
      auto expand = [&](int st) {
        float k[8];
        if (st == 0) {
#pragma unroll
          for (int r = 0; r < 8; ++r) k[r] = c[r];
        } else if (st == 1) {
          k[0] = k[1] = k[2] = c[8]; k[3] = k[4] = k[5] = c[9]; k[6] = k[7] = c[10];
        } else {
          const float lo = c[7 + 2 * st], hi = c[8 + 2 * st];
          k[0] = k[1] = k[2] = k[3] = lo; k[4] = (lane & 1) ? hi : lo; k[5] = k[6] = k[7] = hi;
        }
        const unsigned e0 = pk(k[0], k[1]), e1 = pk(k[2], k[3]), e2 = pk(k[4], k[5]), e3 = pk(k[6], k[7]);
        const unsigned o0 = pk(k[1], k[2]), o1 = pk(k[3], k[4]), o2 = pk(k[5], k[6]);
        const unsigned pe = __builtin_amdgcn_update_dpp(pe3, e3, 0x138, 0xf, 0xf, false), po = __builtin_amdgcn_update_dpp(po2, o2, 0x138, 0xf, 0xf, false);
        pe3 = __builtin_amdgcn_readlane(e3, 63); po2 = __builtin_amdgcn_readlane(o2, 63);
        const unsigned om = __builtin_amdgcn_alignbit(e0, pe, 16);
        T[st][0] = (u32x4){e0, e1, om, o0}; T[st][1] = (u32x4){pe, e0, po, om}; T[st][2] = (u32x4){e2, e3, o1, o2}; T[st][3] = (u32x4){e1, e2, o0, o1};
#pragma unroll
        for (int j = 0; j < 4; ++j) OPQ(T[st][j]);
      };
      WAITL();
      if (lane < 8) *(LAS u32x4*)(tb + lane * 16) = (u32x4){0, 0, 0, 0};
#pragma unroll 1
      for (int i = 0; i < 2; ++i) {
        __builtin_amdgcn_s_barrier(); FENCE();
        if (i == late) {
          int ln = lane; OPQ(ln);
          const int g = ln >> 4, e = (ln >> 2) & 3, pp = ln & 3;
          const unsigned uv = (g >> 1) * 16384 + (2 * ((g & 1) * 4 + e) + (pp >> 1)) * 32 + (((pp ^ g) & 1) << 4);
          const unsigned ta = late * SLOT + (16 * ((g & 1) * 4 + pp) + e) * 128 + (g >> 1) * 8, tx = (2 * c4) ^ (e >> 1);
#pragma unroll
          for (int q2 = 0; q2 < 2; ++q2) {
            const u32x4 A = ldtr2(ta + ((tx ^ q2) << 4), ta + 512 + ((tx ^ q2 ^ 2) << 4)), B = ldtr2(ta + 1024 + ((tx ^ q2 ^ 4) << 4), ta + 1536 + ((tx ^ q2 ^ 6) << 4));
            u32x4 P0, P1;
#pragma unroll
            for (int j = 0; j < 4; ++j) {
              const unsigned sa = __builtin_amdgcn_mov_dpp(A[j], 0x4E, 0xf, 0xf, true), sbb = __builtin_amdgcn_mov_dpp(B[j], 0x4E, 0xf, 0xf, true);
              P0[j] = pp < 2 ? A[j] : sbb;
              P1[j] = pp < 2 ? sa : B[j];
            }
            const unsigned so = WS_U + (8 * c16 + 4 * q2) * 8192 + rt * 512;
            st128(P0, uv, rws, so);
            st128(P1, uv, rws, so + 8192);
          }
        } else {
#pragma unroll
          for (int j = 0; j < 3; ++j) {
            const f32x4 t = *(LAS f32x4*)(kx + j * 1024 + lane * 16);
#pragma unroll
            for (int r = 0; r < 4; ++r) c[4 * j + r] = add1(c[4 * j + r], t[r]);
            c[12 + j] = add1(c[12 + j], *(LAS float*)(kx + 3072 + j * 256 + lane * 4));
          }
        }
      }
      STAMP(20);
      SB(); __builtin_amdgcn_s_barrier(); SB();
      expand(0); expand(1);
      publish(flag(ws, WS_UF, c16 * 16 + rt), tag0, lane);
      STAMP(21);
      SB(); __builtin_amdgcn_s_barrier(); SB();
      expand(2); expand(3);
      STAMP(22);
      SB(); __builtin_amdgcn_s_barrier(); STAMP(23); __builtin_amdgcn_s_barrier(); STAMP(24); SB();
      const GAS u64* const uf = flag(ws, WS_UF, (b >> 3) * 16 + (lane & 15));
      u64 uft = __hip_atomic_load(uf, RLX);
      SB(); __builtin_amdgcn_s_barrier(); SB();
      STAMP(1);
      OPQ(uft);
      const bool early = __all(uft == tag0);
      if (!early) { __builtin_amdgcn_s_barrier(); poll(uf, tag0); }
      FENCE();
      STAMP(2);
      u32x4 uu[4];
#pragma unroll
      for (int j = 0; j < 4; ++j) uu[j] = __builtin_amdgcn_raw_buffer_load_b128(wsr, lane * 16, WS_U + b * 8192 + ((w - 6) * 4 + j) * 1024, 16);
      if (early) __builtin_amdgcn_s_barrier();
      FENCE();
      STAMP(25);
#pragma unroll
      for (int j = 0; j < 4; ++j) {
        unsigned a = tw + ((16 * j) ^ sx); OPQ(a);
#pragma unroll
        for (int st = 0; st < 4; ++st) *(LAS u32x4*)(a + st * 4096) = T[st][j];
      }
      if (w == 6) *(LAS u32x4*)(UB + lane * 16) = (u32x4){0, 0, 0, 0};
#pragma unroll
      for (int j = 0; j < 4; ++j) *(LAS u32x4*)(UB + 1024 + ((w - 6) * 4 + j) * 1024 + lane * 16) = uu[j];
      lset(F_KD + 4 * (w - 6), 2);
      STAMP(8);
  }
  {
    const int ch = w & 1, ts = (w - 4) >> 1, d = 2 * b + ch;
    const float dskip = bf_lo(((const GAS unsigned short*)mk64(kb[2], kb[3]))[d]);
    lwait(F_KD + l1, 2);
    STAMP(3);
    const u64 tag = ldtag();
    const int q = lane >> 4, c = lane & 15;
    f32x4 acc[4];
    {
      const int o = 32 + 2 * c + (q >> 1);
      const unsigned ua = UB + o * 32 + ((ch ^ ((o >> 3) & 1)) << 4) + (q & 1) * 8;
#pragma unroll
      for (int t = 0; t < 4; ++t) {
        const u32x2 uv = lds64(ua + (2 * t + ((t ^ ts) & 1)) * 1024);
        acc[t] = (f32x4){bf_lo(uv[0]), bf_hi(uv[0]), bf_lo(uv[1]), bf_hi(uv[1])} * dskip;
        OPQ(acc[t]);
      }
    }
    if (ts) conv_body<1>(TB0 + ch * TSZ, ch, lane, acc); else conv_body<0>(TB0 + ch * TSZ, ch, lane, acc);
    STAMP(4);
#pragma unroll
    for (int t = 0; t < 4; ++t)
      st128((u32x4){pk(acc[t][0], acc[t][1]), (unsigned)tag, pk(acc[t][2], acc[t][3]), (unsigned)tag}, (c >> 2) * 256 + ((4 * q + (c & 3)) << 4), rws,
            WS_A + d * 8192 + (2 * t + ((t ^ ts) & 1)) * 1024);
    lset(F_H + 4 * (w - 4), 1);
    STAMP(5);
    if (b == 0 && w == 4) {
#pragma unroll 1
      for (unsigned spins = 0; spins < SPIN; ++spins) {
        bool ok = true;
#pragma unroll
        for (int j = 0; j < 4; ++j) ok &= __hip_atomic_load(flag(ws, WS_UF, j * 128 + q * 32 + c), RLX) == tag;
        if (__all(ok)) break;
      }
      if (lane == 0) __hip_atomic_store((GAS u64*)ws, tag, RLX);
    }
    lwait(F_G + (lane & 7) * 4, (lane & 4) ? 1 : 12);
    STAMP(6);
    u32x4 av[4];
#pragma unroll 1
    for (unsigned spins = 0; spins < SPIN; ++spins) {
      bool ok = true;
      FENCE();
#pragma unroll
      for (int j = 0; j < 4; ++j) av[j] = __builtin_amdgcn_raw_buffer_load_b128(wsr, q * 8192 + c * 16, WS_A + (n2 * 64 + (w - 4) * 16 + j * 4) * 8192 + m2 * 256, 16);
#pragma unroll
      for (int j = 0; j < 4; ++j) ok &= av[j][1] == (unsigned)tag && av[j][3] == (unsigned)tag;
      if (__all(ok)) break;
      __builtin_amdgcn_s_sleep(2);
    }
#pragma unroll
    for (int j = 0; j < 4; ++j) *(LAS u32x2*)(AT + ((w - 4) * 16 + j * 4 + q) * 128 + (4 * (c & 3) + (c >> 2)) * 8) = (u32x2){av[j][0], av[j][2]};
    STAMP(7);
    BAR();
  }
  STAMP_END();
}

extern "C" void kernel_launch(void* const* d_in, const int* in_sizes, int n_in, void* d_out, int out_size, void* d_ws, size_t ws_size,
                              hipStream_t stream) {
  (void)hipFuncSetAttribute((const void*)fused_kernel, hipFuncAttributeMaxDynamicSharedMemorySize, LDS_BYTES);
  const Args a = {{d_in[0], d_in[8], d_in[9], d_in[10], d_in[1], d_in[2], d_in[3], d_in[4], d_in[5], d_in[6], d_in[11], d_out, d_ws}};
  fused_kernel<<<256, 512, LDS_BYTES, stream>>>(a);
}
```

```cpp
#include <hip/hip_runtime.h>
#include <stdint.h>
#pragma clang diagnostic ignored "-Wint-to-pointer-cast"

typedef unsigned long long u64;
typedef __bf16 bf16x8 __attribute__((ext_vector_type(8)));
typedef __bf16 bf16x2 __attribute__((ext_vector_type(2)));
typedef short s16x4 __attribute__((ext_vector_type(4)));
typedef short s16x8 __attribute__((ext_vector_type(8)));
typedef float f32x2 __attribute__((ext_vector_type(2)));
typedef float f32x4 __attribute__((ext_vector_type(4)));
typedef float f32x16 __attribute__((ext_vector_type(16)));
typedef unsigned u32x2 __attribute__((ext_vector_type(2)));
typedef unsigned u32x4 __attribute__((ext_vector_type(4)));
#define LAS __attribute__((address_space(3)))
#define GAS __attribute__((address_space(1)))
#define DI __device__ __forceinline__

constexpr int L_ = 2048, D_ = 512, F_ = 1376;
constexpr unsigned SLOT = 40992, WOFF = 16384, WUP = 12320;
constexpr unsigned WLIN = 64, UB = 3 * SLOT - 9216;
constexpr unsigned TB0 = 3 * SLOT, TSZ = 16512;
constexpr unsigned AT = TB0, XCH = TB0 + 8192, STG = XCH + 16384;
constexpr unsigned MISC = TB0 + 2 * TSZ;
constexpr unsigned F_LAND = MISC + 16, F_KD = MISC + 24, F_G = MISC + 32, F_H = MISC + 48, KX = MISC + 64, GWL = SLOT + WOFF;
constexpr unsigned LDS_BYTES = 163840;
constexpr unsigned WS_G = 4096, WS_UF = 16384, WS_HF = WS_UF + 65536, WS_AF = WS_HF + 131072;
constexpr unsigned WS_U = 524288, WS_A = WS_U + 2097152, WS_H = WS_A + 2097152, WS_END = WS_H + 64 * 88064;
constexpr unsigned WS_ST = 16u << 20;
constexpr unsigned CODE_LINES = 152;
#define ROT 11

#define STAMP_INIT()
#define STAMP(i)
#define STAMP_END()

DI float bf_lo(unsigned v) { return __uint_as_float(v << 16); }
DI float bf_hi(unsigned v) { return __uint_as_float(v & 0xffff0000u); }
DI unsigned pk(float a, float b) { f32x2 t = {a, b}; return __builtin_bit_cast(unsigned, __builtin_convertvector(t, bf16x2)); }
DI float rbf(float a) { return bf_lo(pk(a, a)); }
DI u32x4 lds128(unsigned a) { return *(LAS u32x4*)a; }
DI u32x2 lds64(unsigned a) { return *(LAS u32x2*)a; }
DI u32x2 ldtr(unsigned a) { return __builtin_bit_cast(u32x2, __builtin_amdgcn_ds_read_tr16_b64_v4i16((LAS s16x4*)a)); }
DI u32x4 ldtr2(unsigned a, unsigned b) { u32x2 x = ldtr(a), y = ldtr(b); return (u32x4){x[0], x[1], y[0], y[1]}; }
DI f32x16 mfma32(u32x4 a, u32x4 b, f32x16 c) { return __builtin_amdgcn_mfma_f32_32x32x16_bf16(__builtin_bit_cast(bf16x8, a), __builtin_bit_cast(bf16x8, b), c, 0, 0, 0); }
DI f32x4 mfma16(u32x4 a, u32x4 b, f32x4 c) { return __builtin_amdgcn_mfma_f32_16x16x32_bf16(__builtin_bit_cast(bf16x8, a), __builtin_bit_cast(bf16x8, b), c, 0, 0, 0); }
DI void mfma16a(f32x4& c, u32x4 a, u32x4 b) { asm volatile("v_mfma_f32_16x16x32_bf16 %0, %1, %2, %0" : "+v"(c) : "v"(a), "v"(b)); }
DI u32x4 mkrsrc(const void* p, unsigned bytes) { u64 a = (u64)p; return (u32x4){(unsigned)a, (unsigned)(a >> 32) & 0xffffu, bytes, 0x00020000u}; }
DI void dma(unsigned dst, unsigned voff, u32x4 rs, unsigned soff) {
  asm volatile("s_mov_b32 m0, %0\n\ts_nop 0\n\tbuffer_load_dwordx4 %1, %2, %3 offen lds" ::"s"(dst), "v"(voff), "s"(rs), "s"(soff) : "memory");
}
DI void dma4(unsigned dst, unsigned v0, unsigned v1, unsigned v2, unsigned v3, u32x4 rs, unsigned soff) {
  asm volatile("s_mov_b32 m0, %0\n\ts_nop 0\n\tbuffer_load_dwordx4 %1, %5, %6 offen lds\n\tbuffer_load_dwordx4 %2, %5, %6 offen offset:1024 lds\n\t"
               "buffer_load_dwordx4 %3, %5, %6 offen offset:2048 lds\n\tbuffer_load_dwordx4 %4, %5, %6 offen offset:3072 lds"
               ::"s"(dst), "v"(v0), "v"(v1), "v"(v2), "v"(v3), "s"(rs), "s"(soff) : "memory");
}
DI void st128(u32x4 v, unsigned voff, u32x4 rs, unsigned soff) {
  asm volatile("buffer_store_dwordx4 %0, %1, %2, %3 offen sc1\n\ts_nop 1" ::"v"(v), "v"(voff), "s"(rs), "s"(soff) : "memory");
}
#define WAITV(n) asm volatile("s_waitcnt vmcnt(" #n ")" ::: "memory")
#define WAITL() asm volatile("s_waitcnt lgkmcnt(0)" ::: "memory")
#define FENCE() asm volatile("" ::: "memory")
#define OPQ(x) asm volatile("" : "+v"(x))
#define SB() __builtin_amdgcn_sched_barrier(0)
#define BAR() do { asm volatile("s_waitcnt lgkmcnt(0)" ::: "memory"); __builtin_amdgcn_s_barrier(); asm volatile("" ::: "memory"); } while (0)
#define RLX __ATOMIC_RELAXED, __HIP_MEMORY_SCOPE_AGENT
constexpr unsigned SPIN = 1u << 16;
DI GAS u64* flag(char* ws, unsigned base, unsigned i) { return (GAS u64*)(ws + (base + i * 128)); }
DI void poll(const GAS u64* p, u64 tag) {
#pragma unroll 1
  for (unsigned spins = 0; spins < SPIN; ++spins) {
    u64 v = __hip_atomic_load(p, RLX);
    if (__all(v == tag)) break;
    __builtin_amdgcn_s_sleep(2);
  }
  FENCE();
}
DI void publish(GAS u64* p, u64 tag, int lane) {
  WAITV(0);
  if (lane == 0) __hip_atomic_store(p, tag, RLX);
}
DI void lset(unsigned a, unsigned v) { FENCE(); *(volatile LAS unsigned*)a = v; FENCE(); }
DI unsigned lget(unsigned a) { return *(volatile LAS unsigned*)a; }
DI void lwait(unsigned a, unsigned v) {
  unsigned t, n;
  asm volatile("s_mov_b32 %1, 0x400000\n"
               "1:\n\tds_read_b32 %0, %2\n\ts_waitcnt lgkmcnt(0)\n\tv_cmp_gt_u32 vcc, %3, %0\n\ts_and_b64 vcc, exec, vcc\n\ts_cbranch_vccz 2f\n\t"
               "s_sleep 1\n\ts_sub_u32 %1, %1, 1\n\ts_cbranch_scc0 1b\n2:"
               : "=&v"(t), "=&s"(n) : "v"(a), "v"(v) : "vcc", "scc", "memory");
}
DI u64 ldtag() { u32x2 t = lds64(MISC); return ((u64)(unsigned)__builtin_amdgcn_readfirstlane(t[1]) << 32) | (unsigned)__builtin_amdgcn_readfirstlane(t[0]); }

DI float add1(float a, float b) { float r; asm("v_add_f32 %0, %1, %2" : "=v"(r) : "v"(a), "v"(b)); return r; }
struct cw { f32x2 a, b; };
DI cw mkw(f32x2 w) { return cw{w, (f32x2){-w.y, w.x}}; }
DI f32x2 cmul(f32x2 v, cw w) { return __builtin_elementwise_fma(v.yy, w.b, v.xx * w.a); }
DI f32x2 cexp(float zr2, float zv, float p) {
  const float m = __builtin_amdgcn_exp2f(zr2 * p), a = __builtin_amdgcn_fractf(zv * p);
  return (f32x2){__builtin_amdgcn_cosf(a), __builtin_amdgcn_sinf(a)} * m;
}

template <int TS>
DI void conv_body(unsigned tb, int ch, int lane, f32x4 (&acc)[4]) {
  constexpr int T0 = TS ? 1 : 0, T1 = TS ? 2 : 3, T2 = TS ? 5 : 4, T3 = TS ? 6 : 7;
  const int q = lane >> 4, e = (lane >> 2) & 3, pp = lane & 3, c = lane & 15;
  unsigned a0[2], a1[2];
#pragma unroll
  for (int r = 0; r < 2; ++r) {
    const int y0 = 8 * r + 8 - 2 * q + pp, y1 = y0 - 1;
    a0[r] = tb + (y0 >> 1) * 64 + (((((y0 & 1) << 1) | (e >> 1)) ^ ((y0 >> 2) & 3)) << 4) + (e & 1) * 8;
    a1[r] = tb + (y1 >> 1) * 64 + (((((y1 & 1) << 1) | (e >> 1)) ^ ((y1 >> 2) & 3)) << 4) + (e & 1) * 8;
  }
  int o = 30 + 2 * c + q;
  auto baddr = [&]() { const unsigned a = UB + o * 32 + ((ch ^ ((o >> 3) & 1)) << 4); o -= 4; return a; };
  u32x4 A[2][T3 + 1], B[2][T3 + 1];
  unsigned ab = baddr();
  OPQ(ab); OPQ(a0[0]); OPQ(a1[0]);
#pragma unroll
  for (int p = 0; p <= T3; ++p) { A[0][p] = ldtr2(a0[0] + p * 2048, a1[0] + p * 2048); B[0][p] = lds128(ab + p * 1024); }
#pragma unroll 1
  for (int it = 0; it < 4; ++it) {
    if (TS == 0 && it == 1) { WAITV(0); lset(F_LAND + 4 * ch, 9); }
#pragma unroll
    for (int hf = 0; hf < 2; ++hf) {
      if (hf) { a0[0] += 512; a1[0] += 512; }
      ab = baddr();
      OPQ(ab); OPQ(a0[hf ^ 1]); OPQ(a1[hf ^ 1]);
      int ri = 0;
      auto rd = [&]() {
        if (ri <= 2 * T3 + 1) {
          const int p = ri >> 1;
          if (ri & 1) A[hf ^ 1][p] = ldtr2(a0[hf ^ 1] + p * 2048, a1[hf ^ 1] + p * 2048);
          else B[hf ^ 1][T3 - p] = lds128(ab + (T3 - p) * 1024);
        }
        ++ri;
        SB();
      };
      SB();
#pragma unroll
      for (int p = 0; p <= T3; ++p) {
        mfma16a(acc[3], A[hf][p], B[hf][T3 - p]); SB(); rd();
        if (p <= T2) { mfma16a(acc[2], A[hf][p], B[hf][T2 - p < 0 ? 0 : T2 - p]); SB(); rd(); }
        if (p >= T3 - T1) { mfma16a(acc[1], A[hf][T3 - p], B[hf][T1 - T3 + p < 0 ? 0 : T1 - T3 + p]); SB(); rd(); }
        if (p >= T3 - T0) { mfma16a(acc[0], A[hf][T3 - p], B[hf][T0 - T3 + p < 0 ? 0 : T0 - T3 + p]); SB(); rd(); }
      }
      if (!hf) { a0[1] += 512; a1[1] += 512; }
    }
  }
  asm volatile("s_nop 7" : "+v"(acc[0]), "+v"(acc[1]), "+v"(acc[2]), "+v"(acc[3]));
}

typedef unsigned u32x8 __attribute__((ext_vector_type(8)));
typedef unsigned u32x16 __attribute__((ext_vector_type(16)));
DI u64 mk64(unsigned lo, unsigned hi) { return ((u64)hi << 32) | lo; }
struct Args { const void* p[13]; };
#define ARGS_READY() asm volatile("s_waitcnt lgkmcnt(0)" : "+s"(ka), "+s"(kb), "+s"(kc)::"memory"); \
  char* const ws = (char*)mk64(kc[0], kc[1]); \
  const __amdgpu_buffer_rsrc_t wsr = __builtin_amdgcn_make_buffer_rsrc(ws, 0, WS_END, 0x00020000); \
  const u32x4 rwd = {ka[6], ka[7], F_ * D_ * 2, 0x00020000u}, rws = {kc[0], kc[1], WS_END, 0x00020000u};

__global__ __launch_bounds__(512, 2) void fused_kernel(Args) {
  u32x16 ka; u32x8 kb; u32x2 kc;
  asm volatile("s_load_dwordx16 %0, %3, 0x0\n\ts_load_dwordx8 %1, %3, 0x40\n\ts_load_dwordx2 %2, %3, 0x60"
               : "=&s"(ka), "=&s"(kb), "=&s"(kc) : "s"(__builtin_amdgcn_kernarg_segment_ptr()) : "memory");
  SB();
  const int b = blockIdx.x, xcd = b & 7, bi = b >> 3;
  const int lane = threadIdx.x & 63, w = __builtin_amdgcn_readfirstlane(threadIdx.x >> 6);
  unsigned cpf = 0;
  if (w == 2) {
    u64 pc; asm volatile("s_getpc_b64 %0" : "=s"(pc));
    cpf = *(const GAS unsigned*)((pc & ~127ull) + (unsigned)min(bi * 8 + (lane & 7), (int)CODE_LINES - 1) * 128);
    *(LAS unsigned*)(F_LAND + min(lane, 11) * 4) = 0;
  }
  const int rt = (xcd & 1) * 8 + (bi & 7), ct = (xcd >> 1) * 4 + (bi >> 3);
  const int m2 = xcd * 4 + (bi & 3), n2 = bi >> 2;
  const int k0 = (bi + (bi >> 3) * 2) & 7;
  const bool spare = ct == 15;
  const unsigned l1 = (lane & 1) * 4, l3 = (lane & 3) * 4;
  const unsigned wdv = (lane >> 3) * 1024 + (((lane & 7) ^ ((lane >> 2) & 4)) << 4);
  auto kstep = [&](int j) { const int ks = j + (m2 & 3) * ROT; return ks >= 43 ? ks - 43 : ks; };
#define WD_STEP(j, idx) dma4(WLIN + (idx) * 4096, wdv, wdv + 7168, wdv + 14336, wdv + 21504, rwd, kstep(j) * 32768 + n2 * 128)
  unsigned vx[8], vw[12];
#pragma unroll
  for (int a = 0; a < 8; ++a) { vx[a] = (lane >> 3) * 1024 + (((lane & 7) ^ ((4 * a + (lane >> 4)) & 7)) << 4) + a * 8192 - (a & 3) * 1024; OPQ(vx[a]); }
#pragma unroll
  for (int c = 0; c < 12; ++c) {
    const unsigned ee = 64 * (c % 3) + lane;
    vw[c] = (ee / 12) * 2752 + ((ee % 12) & (ct == 14 ? 3 : 15)) * 16 + (c / 3) * 44032 - (c & 3) * 1024; OPQ(vw[c]);
  }
  unsigned po = ((2 * b + (w & 1)) * 64 + lane) * 4; OPQ(po);
  ARGS_READY();
  STAMP_INIT(); STAMP(0);
  const bool kgen = ((w + 2) & 7) < 4;
  unsigned p_lar = 0, p_aim = 0, p_cre = 0, p_cim = 0, p_ldt = 0;
  u64 ep = 0;
  if (kgen) {
    p_lar = *(const GAS unsigned*)(mk64(ka[8], ka[9]) + po); p_aim = *(const GAS unsigned*)(mk64(ka[10], ka[11]) + po);
    p_ldt = ((const GAS unsigned*)mk64(kb[0], kb[1]))[b];
    p_cre = *(const GAS unsigned*)(mk64(ka[12], ka[13]) + po); p_cim = *(const GAS unsigned*)(mk64(ka[14], ka[15]) + po);
    ep = __hip_atomic_load((const GAS u64*)ws, RLX);
  }

  if (__builtin_expect((w >> 1) == 2, 1)) {
      const int j = w - 4;
      const u32x4 rx = {ka[0], ka[1], L_ * D_ * 2, 0x00020000u}, rw = {j ? ka[4] : ka[2], j ? ka[5] : ka[3], D_ * F_ * 2, 0x00020000u};
      if (spare && j == 0) dma(GWL, lane * 16, (u32x4){kb[4], kb[5], 1024, 0x00020000u}, 0);
      unsigned sb = 0;
#pragma unroll 1
      for (int k = 0; k < 8; ++k) {
        const int kt = (k + k0) & 7;
        const unsigned sx = (rt * 128 + 64 * j) * 1024 + kt * 128, sw = kt * 64 * 2752 + ct * 192;
#pragma unroll
        for (int a = 0; a < 8; a += 4) dma4(sb + (8 * j + a) * 1024, vx[a], vx[a + 1], vx[a + 2], vx[a + 3], rx, sx);
        if (!spare) {
#pragma unroll
          for (int c = 0; c < 12; c += 4) dma4(sb + WOFF + j * WUP + c * 1024, vw[c], vw[c + 1], vw[c + 2], vw[c + 3], rw, sw);
          WAITV(20);
        } else WAITV(8);
#define PF(j) "s_load_dword s90, %0, " #j "*32768\n\t"
        if (k >= 2) asm volatile(PF(0) PF(1) PF(2) PF(3) PF(4) PF(5) PF(6) ::"s"(mk64(ka[6], ka[7]) + (2 * b + j) * 64 + (k - 2) * 7 * 32768) : "s90", "memory");
        if (k < 2) STAMP(15 + k);
        if (k) __builtin_amdgcn_s_barrier();
        sb = sb == 2 * SLOT ? 0 : sb + SLOT;
      }
      asm volatile(PF(0) ::"s"(mk64(ka[6], ka[7]) + (2 * b + j) * 64 + 42 * 32768) : "s90", "memory");
#pragma unroll 1
      for (int p = j; p < 8; p += 2) WD_STEP(min(p, 6), 20 + min(p, 6));
      WAITV(16);
      __builtin_amdgcn_s_barrier();
#pragma unroll 1
      for (int p = j; p < 9; p += 2) { WAITV(10); WD_STEP(7 + p, p); }
      WAITL();
      STAMP(1);
  } else {
    f32x16 c;
#pragma unroll
    for (int j = 0; j < 16; ++j) c[j] = 0.f;
    f32x16 acc1[3][2];
#pragma unroll
    for (int g = 0; g < 3; ++g)
#pragma unroll
      for (int t = 0; t < 2; ++t) {
#pragma unroll
        for (int j = 0; j < 16; ++j) acc1[g][t][j] = 0.f;
        OPQ(acc1[g][t]);
      }
    const unsigned tb = TB0 + (w & 1) * TSZ;
    if (kgen) {
      const unsigned kb0 = w < 2 ? 2 * SLOT + w * TSZ : tb;
      const int r31 = lane & 31, sh = w < 2 ? 0 : 16;
      const u64 gt = (r31 & 16) ? ((r31 & 8) ? 0x001a191600181714ull : 0x15121110130f0e0dull) : ((r31 & 8) ? 0x0c0b0a0a07060504ull : 0x0a09080803020100ull);
      unsigned ga = kb0 + (32 + (unsigned)((gt >> (8 * (r31 & 7))) & 31)) * 272 + (lane >> 5) * 16, ea = kb0 + r31 * 272 + (lane >> 5) * 16;
      unsigned wb = kb0 + lane * 4;
      OPQ(ga); OPQ(ea); OPQ(wb);
      const float dt = rbf(__builtin_amdgcn_exp2f(bf_hi(p_ldt << ((w & 1) ? 0 : 16)) * 1.442695041f));
      const float zr2 = -rbf(__builtin_amdgcn_exp2f(bf_hi(p_lar << sh) * 1.442695041f)) * dt * 1.442695041f;
      const float zv = bf_hi(p_aim << sh) * dt * 0.1591549431f;
      STAMP(16);
      {
        const cw w8 = mkw(cexp(zr2, zv, 8.f)), w32 = mkw(cexp(zr2, zv, 32.f));
        f32x2 ev[4];
        ev[0] = (f32x2){1.f, 0.f}; ev[1] = w8.a; ev[2] = cmul(w8.a, w8); ev[3] = cmul(ev[2], w8);
#pragma unroll
        for (int i = 0; i < 32; ++i) {
          *(LAS unsigned*)(wb + i * 272) = pk(ev[i & 3].x, ev[i & 3].y);
          if (i < 28) ev[i & 3] = i ? cmul(ev[i & 3], w32) : w32.a;
        }
      }
      u32x4 fa[8], fb[8];
      FENCE();
#pragma unroll
      for (int s = 0; s < 8; ++s) fb[s] = lds128(ea + s * 32);
      FENCE();
      {
        const cw w1 = mkw(cexp(zr2, -zv, 1.f)), w2 = mkw(cmul(w1.a, w1)), w4 = mkw(cmul(w2.a, w2)), w256 = mkw(cexp(zr2, -zv, 256.f));
        f32x2 g[9];
        g[0] = (f32x2){bf_hi(p_cre << sh), -bf_hi(p_cim << sh)} * (2.f * dt);
        g[1] = cmul(g[0], w1); g[2] = cmul(g[0], w2); g[3] = cmul(g[1], w2);
#pragma unroll
        for (int i = 0; i < 5; ++i) g[4 + i] = cmul(g[i], w4);
        int row = 32;
#pragma unroll
        for (int i = 0; i < 8; ++i) *(LAS unsigned*)(wb + (row++) * 272) = pk(g[i].x, g[i].y);
#pragma unroll
        for (int bk = 1; bk < 8; ++bk) {
          const int stp = bk == 1 ? 2 : (bk < 4 ? 4 : 8);
#pragma unroll
          for (int i = 0; i <= 8; i += stp) { g[i] = cmul(g[i], w256); *(LAS unsigned*)(wb + (row++) * 272) = pk(g[i].x, g[i].y); }
        }
      }
      FENCE();
      STAMP(18);
#pragma unroll
      for (int s = 0; s < 8; ++s) fa[s] = lds128(ga + s * 32);
      SB();
#pragma unroll
      for (int s = 0; s < 8; ++s) c = mfma32(fa[s], fb[s], c);
      FENCE();
    }
    const unsigned kx = KX + (w & 1) * 3840;
    if (w < 4) {
    const int h = lane >> 5, l31 = lane & 31, mh = w & 1, nh = w >> 1;
    if (w < 2) {
#pragma unroll
      for (int j = 0; j < 3; ++j) *(LAS f32x4*)(kx + j * 1024 + lane * 16) = (f32x4){c[4 * j], c[4 * j + 1], c[4 * j + 2], c[4 * j + 3]};
#pragma unroll
      for (int j = 0; j < 3; ++j) *(LAS float*)(kx + 3072 + j * 256 + lane * 4) = c[12 + j];
    }
    WAITL();
    STAMP(19);
    if (!spare) {
      unsigned xo[4];
#pragma unroll
      for (int s = 0; s < 4; ++s) { xo[s] = (64 * mh + l31) * 128 + (((2 * s + h) ^ ((l31 >> 1) & 7)) << 4); OPQ(xo[s]); }
      unsigned wo = WOFF + ((lane >> 4) & 1) * WUP + (8 * h + ((lane >> 2) & 3)) * 192 + nh * 96 + (lane & 1) * 16 + (lane & 2) * 4;
      OPQ(wo);
      f32x16 (&acc)[3][2] = acc1;
      unsigned hso[3][2];
#pragma unroll
      for (int g = 0; g < 3; ++g) {
        const int g16 = ct * 6 + nh * 3 + g, p0 = (g16 >> 1) - ((2 * rt + mh) & 3) * ROT, pos = p0 < 0 ? p0 + 43 : p0;
#pragma unroll
        for (int t = 0; t < 2; ++t) {
          hso[g][t] = g16 < 86 ? WS_H + (rt * 4 + 2 * mh + t) * 88064 + (g16 & 1) * 44032 + pos * 1024 : WS_END;
          asm volatile("" : "+s"(hso[g][t]));
        }
      }
      u32x4 xb[2][2], wa[2][3];
      __builtin_amdgcn_s_barrier(); FENCE();
      STAMP(1);
      xb[0][0] = lds128(xo[0]); wa[0][0] = ldtr2(wo, wo + 768); xb[0][1] = lds128(xo[0] + 4096);
      wa[0][1] = ldtr2(wo + 32, wo + 800); wa[0][2] = ldtr2(wo + 64, wo + 832);
      unsigned sb = 0;
#pragma unroll 1
      for (int k = 0; k < 8; ++k) {
#pragma unroll
        for (int s = 0; s < 4; ++s) {
          const int c = s & 1, n = c ^ 1, sn = (s + 1) & 3;
          auto r0 = [&]() { xb[n][0] = lds128(xo[sn]); wa[n][0] = ldtr2(wo + sn * 3072, wo + sn * 3072 + 768); SB(); };
          auto r1 = [&]() { xb[n][1] = lds128(xo[sn] + 4096); SB(); };
          auto r2 = [&]() { wa[n][1] = ldtr2(wo + sn * 3072 + 32, wo + sn * 3072 + 800); SB(); };
          auto r3 = [&]() { wa[n][2] = ldtr2(wo + sn * 3072 + 64, wo + sn * 3072 + 832); SB(); };
          SB();
          acc[0][0] = mfma32(wa[c][0], xb[c][0], acc[0][0]); SB();
          if (s < 3) r0();
          else {
            const unsigned dl = sb == 2 * SLOT ? -2 * SLOT : SLOT;
            sb += dl; wo += dl;
#pragma unroll
            for (int i = 0; i < 4; ++i) { xo[i] += dl; OPQ(xo[i]); }
            OPQ(wo); SB();
          }
          acc[0][1] = mfma32(wa[c][0], xb[c][1], acc[0][1]); SB();
          if (s < 3) r1();
          else {
            if (k < 7) BAR();
            SB(); r0();
          }
          acc[1][0] = mfma32(wa[c][1], xb[c][0], acc[1][0]); SB();
          if (s < 3) r2(); else r1();
          acc[1][1] = mfma32(wa[c][1], xb[c][1], acc[1][1]); SB();
          if (s < 3) r3(); else r2();
          acc[2][0] = mfma32(wa[c][2], xb[c][0], acc[2][0]); SB();
          if (s == 3) r3();
          acc[2][1] = mfma32(wa[c][2], xb[c][1], acc[2][1]); SB();
        }
      }
      STAMP(2);
      lset(F_G + 4 * w, 8);
#pragma unroll
      for (int g = 0; g < 3; ++g) {
#pragma unroll
        for (int t = 0; t < 2; ++t) {
          u32x4 o;
#pragma unroll
          for (int j = 0; j < 4; ++j) {
            const f32x2 gg = {acc[g][t][2 * j], acc[g][t][2 * j + 1]}, uu = {acc[g][t][8 + 2 * j], acc[g][t][9 + 2 * j]};
            const f32x2 z = gg * __builtin_elementwise_fma(gg * gg, (f32x2){-0.1029432397f, -0.1029432397f}, (f32x2){-2.302208198f, -2.302208198f});
            const f32x2 dn = (f32x2){__builtin_amdgcn_exp2f(z.x), __builtin_amdgcn_exp2f(z.y)} + 1.f;
            const f32x2 hh = gg * uu * (f32x2){__builtin_amdgcn_rcpf(dn.x), __builtin_amdgcn_rcpf(dn.y)};
            o[j] = pk(hh.x, hh.y);
          }
          st128(o, lane * 16, rws, hso[g][t]);
        }
      }
    } else {
      f32x16& ga = acc1[0][0];
      unsigned sb = 0;
#pragma unroll 1
      for (int k = 0; k < 8; ++k) {
        BAR();
#pragma unroll
        for (int s = 0; s < 4; ++s)
          ga = mfma32(lds128(GWL + ((k + k0) & 7) * 128 + s * 32 + h * 16), lds128(sb + (32 * w + l31) * 128 + (((2 * s + h) ^ ((l31 >> 1) & 7)) << 4)), ga);
        sb = sb == 2 * SLOT ? 0 : sb + SLOT;
      }
      const float gate = __builtin_amdgcn_rcpf(1.f + __builtin_amdgcn_exp2f(ga[0] * -1.442695041f));
      if (lane < 32) __hip_atomic_store((GAS float*)(ws + WS_G) + rt * 128 + 32 * w + lane, gate, RLX);
    }
    STAMP(3);
    lset(F_G + 4 * w, 9);
    const u64 tag = ldtag();
    lwait(F_G + l3, 8);
#pragma unroll 1
    for (int p = 9 + w; p < 21; p += 4) WD_STEP(7 + min(p, 19), min(p, 19));
    WAITV(12);
    if (lane == 0) __hip_atomic_store(flag(ws, WS_HF, spare ? (2 * rt + nh) * 32 + 30 + mh : (2 * rt + mh) * 32 + 2 * ct + nh), tag, RLX);
    STAMP(4);
    const int e = (lane >> 2) & 3, pp = lane & 3, blk = (lane >> 4) & 1;
    unsigned wa[2];
#pragma unroll
    for (int dt = 0; dt < 2; ++dt) wa[dt] = WLIN + (16 * nh + 8 * h + e) * 128 + ((dt ^ (e >> 1)) << 6) + (pp & 1) * 32 + blk * 16 + (pp >> 1) * 8;
    lwait(F_LAND + l1, 9);
    STAMP(5);
    u32x4 pre[16][2];
    unsigned wh[2] = {wa[0] + 20 * 4096, wa[1] + 20 * 4096};
    OPQ(wa[0]); OPQ(wa[1]); OPQ(wh[0]); OPQ(wh[1]);
#pragma unroll
    for (int j = 0; j < 16; ++j)
#pragma unroll
      for (int dt = 0; dt < 2; ++dt) { const unsigned a = j < 7 ? wh[dt] + j * 4096 : wa[dt] + (j - 7) * 4096; pre[j][dt] = ldtr2(a, a + 512); }
    lset(F_G + 4 * w, 10);
    lwait(F_G + l3, 10);
#pragma unroll 1
    for (int j = 27 + w; j < 43; j += 4) WD_STEP(j, j < 34 ? j - 7 : j - 34);
    STAMP(6);
    if (w == 0) poll(flag(ws, WS_HF, m2 * 32 + l31), tag);
    WAITV(0);
    lset(F_G + 4 * w, 11);
    lwait(F_G + l3, 11);
    STAMP(7);
    unsigned hb = WS_H + (m2 * 2 + mh) * 88064 + nh * 44032;
    auto ldh = [&](int j) { return __builtin_amdgcn_raw_buffer_load_b128(wsr, lane * 16 + (j & 3) * 1024, hb + (j >> 2) * 4096, 16); };
    u32x4 hr[9];
#pragma unroll
    for (int j = 0; j < 9; ++j) hr[(j + 2) % 9] = ldh(j);
    const float gate = __hip_atomic_load((const GAS float*)(ws + WS_G) + m2 * 64 + mh * 32 + l31, RLX);
    f32x16 acc[2];
#pragma unroll
    for (int j = 0; j < 16; ++j) acc[0][j] = acc[1][j] = 0.f;
    u32x4 wf[3][2];
#pragma unroll
    for (int dt = 0; dt < 2; ++dt) { wa[dt] += 9 * 4096; OPQ(wa[dt]); }
#pragma unroll
    for (int j = 0; j < 16; ++j) {
      SB();
      acc[0] = mfma32(pre[j][0], hr[(j + 2) % 9], acc[0]);
      acc[1] = mfma32(pre[j][1], hr[(j + 2) % 9], acc[1]);
      SB();
      hr[(j + 2) % 9] = ldh(j + 9);
      if (j >= 14) { wf[j - 14][0] = ldtr2(wa[0] + (j - 14) * 4096, wa[0] + (j - 14) * 4096 + 512); wf[j - 14][1] = ldtr2(wa[1] + (j - 14) * 4096, wa[1] + (j - 14) * 4096 + 512); }
    }
    hb += 25 * 1024;
    STAMP(13);
#pragma unroll 1
    for (int it = 0; it < 3; ++it) {
#pragma unroll
      for (int u = 0; u < 9; ++u) {
        if (u == 7) {
          const unsigned dl = it == 1 ? -18 * 4096 : 9 * 4096;
          wa[0] += dl; wa[1] += dl; OPQ(wa[0]); OPQ(wa[1]);
        }
        const int un = (u + 2) % 9;
        SB();
        acc[0] = mfma32(wf[u % 3][0], hr[u], acc[0]); SB();
        wf[un % 3][0] = ldtr2(wa[0] + un * 4096, wa[0] + un * 4096 + 512); SB();
        acc[1] = mfma32(wf[u % 3][1], hr[u], acc[1]); SB();
        wf[un % 3][1] = ldtr2(wa[1] + un * 4096, wa[1] + un * 4096 + 512);
        hr[u] = ldh(u);
      }
      hb = it == 0 ? hb + 9 * 1024 : WS_END;
      lset(F_G + 4 * w, 12);
    }
    STAMP(8);
    lwait(F_H + l3, 1);
    const f32x16 snd = nh ? acc[0] : acc[1], keep = nh ? acc[1] : acc[0];
#pragma unroll
    for (int j = 0; j < 4; ++j) *(LAS f32x4*)(XCH + w * 4096 + j * 1024 + lane * 16) = (f32x4){snd[4 * j], snd[4 * j + 1], snd[4 * j + 2], snd[4 * j + 3]};
    BAR();
    STAMP(9);
    u32x4 o[2];
#pragma unroll
    for (int j = 0; j < 4; ++j) {
      const f32x4 r = *(LAS f32x4*)(XCH + (w ^ 2) * 4096 + j * 1024 + lane * 16);
      const u32x2 av = ldtr(AT + (32 * nh + 16 * h + 4 * j + e) * 128 + ((4 * (pp >> 1) + 2 * mh + blk) << 4) + (pp & 1) * 8);
      const float b0 = keep[4 * j] + r[0], b1 = keep[4 * j + 1] + r[1], b2 = keep[4 * j + 2] + r[2], b3 = keep[4 * j + 3] + r[3];
      o[j >> 1][2 * (j & 1)] = pk(b0 + gate * (bf_lo(av[0]) - b0), b1 + gate * (bf_hi(av[0]) - b1));
      o[j >> 1][2 * (j & 1) + 1] = pk(b2 + gate * (bf_lo(av[1]) - b2), b3 + gate * (bf_hi(av[1]) - b3));
    }
    const unsigned st = STG + w * 2048;
#pragma unroll
    for (int t = 0; t < 2; ++t) *(LAS u32x4*)(st + l31 * 64 + (((2 * h + t) ^ ((l31 >> 1) & 3)) << 4)) = o[t];
    FENCE();
    const u32x4 rout = {kb[6], kb[7], L_ * D_ * 2, 0x00020000u};
#pragma unroll
    for (int t = 0; t < 2; ++t) {
      const int row = 16 * t + (lane >> 2);
      st128(lds128(st + row * 64 + (((lane & 3) ^ ((row >> 1) & 3)) << 4)), row * 1024 + (lane & 3) * 16, rout, (m2 * 64 + mh * 32) * 1024 + n2 * 128 + nh * 64);
    }
    asm volatile("" ::"v"(cpf));
    STAMP(10);
    STAMP_END();
    __builtin_amdgcn_endpgm();
    }
      OPQ(ep);
      const u64 tag0 = ep * 6364136223846793005ull + 1442695040888963407ull;
      *(LAS u32x2*)MISC = (u32x2){(unsigned)tag0, (unsigned)(tag0 >> 32)};
      STAMP(19);
      const int late = xcd >> 2, c4 = (xcd & 2) + (w & 1), c16 = ((k0 + late) & 7) * 4 + c4;
      unsigned pe3 = 0, po2 = 0;
      unsigned tw = tb + 128 + lane * 64;
      const unsigned sx = (((lane >> 1) + 1) & 3) << 4;
      u32x4 T[4][4];
      auto expand = [&](int st) {
        float k[8];
        if (st == 0) {
#pragma unroll
          for (int r = 0; r < 8; ++r) k[r] = c[r];
        } else if (st == 1) {
          k[0] = k[1] = k[2] = c[8]; k[3] = k[4] = k[5] = c[9]; k[6] = k[7] = c[10];
        } else {
          const float lo = c[7 + 2 * st], hi = c[8 + 2 * st];
          k[0] = k[1] = k[2] = k[3] = lo; k[4] = (lane & 1) ? hi : lo; k[5] = k[6] = k[7] = hi;
        }
        const unsigned e0 = pk(k[0], k[1]), e1 = pk(k[2], k[3]), e2 = pk(k[4], k[5]), e3 = pk(k[6], k[7]);
        const unsigned o0 = pk(k[1], k[2]), o1 = pk(k[3], k[4]), o2 = pk(k[5], k[6]);
        const unsigned pe = __builtin_amdgcn_update_dpp(pe3, e3, 0x138, 0xf, 0xf, false), po = __builtin_amdgcn_update_dpp(po2, o2, 0x138, 0xf, 0xf, false);
        pe3 = __builtin_amdgcn_readlane(e3, 63); po2 = __builtin_amdgcn_readlane(o2, 63);
        const unsigned om = __builtin_amdgcn_alignbit(e0, pe, 16);
        T[st][0] = (u32x4){e0, e1, om, o0}; T[st][1] = (u32x4){pe, e0, po, om}; T[st][2] = (u32x4){e2, e3, o1, o2}; T[st][3] = (u32x4){e1, e2, o0, o1};
#pragma unroll
        for (int j = 0; j < 4; ++j) OPQ(T[st][j]);
      };
      WAITL();
      if (lane < 8) *(LAS u32x4*)(tb + lane * 16) = (u32x4){0, 0, 0, 0};
#pragma unroll 1
      for (int i = 0; i < 2; ++i) {
        __builtin_amdgcn_s_barrier(); FENCE();
        if (i == late) {
          const int g = lane >> 4, e = (lane >> 2) & 3, pp = lane & 3;
          const unsigned uv = (g >> 1) * 16384 + (2 * ((g & 1) * 4 + e) + (pp >> 1)) * 32 + (((pp ^ g) & 1) << 4);
          const unsigned ta = late * SLOT + (16 * ((g & 1) * 4 + pp) + e) * 128 + (g >> 1) * 8, tx = (2 * c4) ^ (e >> 1);
#pragma unroll
          for (int q2 = 0; q2 < 2; ++q2) {
            const u32x4 A = ldtr2(ta + ((tx ^ q2) << 4), ta + 512 + ((tx ^ q2 ^ 2) << 4)), B = ldtr2(ta + 1024 + ((tx ^ q2 ^ 4) << 4), ta + 1536 + ((tx ^ q2 ^ 6) << 4));
            u32x4 P0, P1;
#pragma unroll
            for (int j = 0; j < 4; ++j) {
              const unsigned sa = __builtin_amdgcn_mov_dpp(A[j], 0x4E, 0xf, 0xf, true), sbb = __builtin_amdgcn_mov_dpp(B[j], 0x4E, 0xf, 0xf, true);
              P0[j] = pp < 2 ? A[j] : sbb;
              P1[j] = pp < 2 ? sa : B[j];
            }
            const unsigned so = WS_U + (8 * c16 + 4 * q2) * 8192 + rt * 512;
            st128(P0, uv, rws, so);
            st128(P1, uv, rws, so + 8192);
          }
        } else {
#pragma unroll
          for (int j = 0; j < 3; ++j) {
            const f32x4 t = *(LAS f32x4*)(kx + j * 1024 + lane * 16);
#pragma unroll
            for (int r = 0; r < 4; ++r) c[4 * j + r] = add1(c[4 * j + r], t[r]);
            c[12 + j] = add1(c[12 + j], *(LAS float*)(kx + 3072 + j * 256 + lane * 4));
          }
        }
      }
      STAMP(20);
      SB(); __builtin_amdgcn_s_barrier(); SB();
      expand(0); expand(1);
      publish(flag(ws, WS_UF, c16 * 16 + rt), tag0, lane);
      STAMP(21);
      SB(); __builtin_amdgcn_s_barrier(); SB();
      expand(2); expand(3);
      STAMP(22);
      SB(); __builtin_amdgcn_s_barrier(); STAMP(23); __builtin_amdgcn_s_barrier(); STAMP(24); SB();
      const GAS u64* const uf = flag(ws, WS_UF, (b >> 3) * 16 + (lane & 15));
      u64 uft = __hip_atomic_load(uf, RLX);
      SB(); __builtin_amdgcn_s_barrier(); SB();
      STAMP(1);
      OPQ(uft);
      const bool early = __all(uft == tag0);
      if (!early) { __builtin_amdgcn_s_barrier(); poll(uf, tag0); }
      FENCE();
      STAMP(2);
      u32x4 uu[4];
#pragma unroll
      for (int j = 0; j < 4; ++j) uu[j] = __builtin_amdgcn_raw_buffer_load_b128(wsr, lane * 16, WS_U + b * 8192 + ((w - 6) * 4 + j) * 1024, 16);
      if (early) __builtin_amdgcn_s_barrier();
      FENCE();
      STAMP(25);
#pragma unroll
      for (int j = 0; j < 4; ++j) {
        unsigned a = tw + ((16 * j) ^ sx); OPQ(a);
#pragma unroll
        for (int st = 0; st < 4; ++st) *(LAS u32x4*)(a + st * 4096) = T[st][j];
      }
      if (w == 6) *(LAS u32x4*)(UB + lane * 16) = (u32x4){0, 0, 0, 0};
#pragma unroll
      for (int j = 0; j < 4; ++j) *(LAS u32x4*)(UB + 1024 + ((w - 6) * 4 + j) * 1024 + lane * 16) = uu[j];
      lset(F_KD + 4 * (w - 6), 2);
      STAMP(8);
  }
  {
    const int ch = w & 1, ts = (w - 4) >> 1, d = 2 * b + ch;
    const float dskip = bf_lo(((const GAS unsigned short*)mk64(kb[2], kb[3]))[d]);
    lwait(F_KD + (lane & 7) * 4, (lane & 6) == 0 ? 2 : ((lane & 6) == 6 ? 0 : 9));
    STAMP(3);
    f32x4 acc[4];
#pragma unroll
    for (int t = 0; t < 4; ++t) acc[t] = (f32x4){0.f, 0.f, 0.f, 0.f};
    if (ts) conv_body<1>(TB0 + ch * TSZ, ch, lane, acc); else conv_body<0>(TB0 + ch * TSZ, ch, lane, acc);
    STAMP(4);
    lset(F_H + 4 * (w - 4), 1);
    {
      const int q = lane >> 4, c = lane & 15;
      const int o = 32 + 2 * c + (q >> 1);
      const unsigned ua = UB + o * 32 + ((ch ^ ((o >> 3) & 1)) << 4) + (q & 1) * 8;
      u32x2 pa[4];
#pragma unroll
      for (int t = 0; t < 4; ++t) {
        const int tt = 2 * t + ((t ^ ts) & 1);
        const u32x2 uv = lds64(ua + tt * 1024);
        pa[t] = (u32x2){pk(acc[t][0] + bf_lo(uv[0]) * dskip, acc[t][1] + bf_hi(uv[0]) * dskip), pk(acc[t][2] + bf_lo(uv[1]) * dskip, acc[t][3] + bf_hi(uv[1]) * dskip)};
      }
      const int tl = (q & 1) ? 3 - ts : ts;
      const unsigned av = (4 * tl + (c >> 2)) * 128 + ((4 * (q >> 1) + (c & 3)) << 4);
#pragma unroll
      for (int t = 0; t < 2; ++t) {
        const auto s0 = __builtin_amdgcn_permlane16_swap(pa[2 * t][0], pa[2 * t + 1][0], false, false);
        const auto s1 = __builtin_amdgcn_permlane16_swap(pa[2 * t][1], pa[2 * t + 1][1], false, false);
        st128((u32x4){s0[0], s1[0], s0[1], s1[1]}, av, rws, WS_A + d * 4096 + t * 2048);
      }
    }
    const u64 tag = ldtag();
    publish(flag(ws, WS_AF, ts * 512 + d), tag, lane);
    STAMP(5);
    {
      const int a = w - 4, t = m2 >> 2, tsn = (t ^ (t >> 1)) & 1;
      lwait(F_G + l3, 12);
      poll(flag(ws, WS_AF, tsn * 512 + n2 * 64 + a * 16 + (lane & 15)), tag);
      STAMP(6);
      u32x4 av[2];
#pragma unroll
      for (int j = 0; j < 2; ++j) av[j] = __builtin_amdgcn_raw_buffer_load_b128(wsr, (lane >> 3) * 4096 + (lane & 7) * 16, WS_A + (n2 * 64 + a * 16 + j * 8) * 4096 + m2 * 128, 16);
      lwait(F_H + l3, 1);
#pragma unroll
      for (int j = 0; j < 2; ++j) *(LAS u32x4*)(AT + (a * 2 + j) * 1024 + lane * 16) = av[j];
    }
    STAMP(7);
    BAR();
    if (b == 0 && w == 4) {
#pragma unroll 1
      for (int j = 0; j < 4; ++j) poll(flag(ws, WS_AF, 2 * (j * 64 + lane)), tag);
      if (lane == 0) __hip_atomic_store((GAS u64*)ws, tag, RLX);
    }
  }
  STAMP_END();
}

extern "C" void kernel_launch(void* const* d_in, const int* in_sizes, int n_in, void* d_out, int out_size, void* d_ws, size_t ws_size,
                              hipStream_t stream) {
  (void)hipFuncSetAttribute((const void*)fused_kernel, hipFuncAttributeMaxDynamicSharedMemorySize, LDS_BYTES);
  const Args a = {{d_in[0], d_in[8], d_in[9], d_in[10], d_in[1], d_in[2], d_in[3], d_in[4], d_in[5], d_in[6], d_in[11], d_out, d_ws}};
  fused_kernel<<<256, 512, LDS_BYTES, stream>>>(a);
}
```

```cpp
#include <hip/hip_runtime.h>
typedef unsigned long long u64;
constexpr unsigned WS_G = 4096, WS_U = 524288, WS_A = 524288, WS_H = 2621440, WS_END = 8257536, WS_ST = 16777216;
constexpr int ROT = 0;
struct Buf { const void* p; unsigned bytes, fmt; };
struct Args { Buf x, wg, ws, wu, wd; const void* par[6]; Buf out, gw; };

__global__ __launch_bounds__(512, 2) void fused_kernel(Args) {
  asm volatile(
    ".Lk_entry:\n"
    "s_load_dwordx16 s[4:19], s[0:1], 0\n"
    "s_load_dwordx16 s[24:39], s[0:1], 64\n"
    "v_readfirstlane_b32 s40, v0\n"
    "v_and_b32 v1, 63, v0\n"
    "s_lshr_b32 s40, s40, 6\n"
    "s_and_b32 s41, s40, 1\n"
    "s_cmp_eq_u32 s40, 2\n"
    "s_cbranch_scc0 .Lk_nopf\n"
    "s_getpc_b64 s[50:51]\n"
    "v_bfi_b32 v2, -8, s2, v0\n"
    "s_and_b32 s50, s50, 4294967168\n"
    "v_min_u32 v2, (.Lk_end-.Lk_entry)/128-1, v2\n"
    "v_lshlrev_b32 v2, 7, v2\n"
    "global_load_dword v255, v2, s[50:51]\n"
    "v_min_u32 v2, 11, v1\n"
    "v_lshlrev_b32 v2, 2, v2\n"
    "v_add_u32 v2, 163792, v2\n"
    "v_mov_b32 v3, 0\n"
    "ds_write_b32 v2, v3\n"
    ".Lk_nopf:\n"
    "s_lshr_b32 s43, s2, 3\n"
    "s_lshl_b32 s50, s2, 3\n"
    "s_and_b32 s50, s50, 8\n"
    "s_and_b32 s44, s43, 7\n"
    "s_or_b32 s44, s44, s50\n"
    "s_lshr_b32 s51, s2, 6\n"
    "s_lshl_b32 s50, s2, 2\n"
    "s_xor_b32 s50, s50, s2\n"
    "s_bfe_u32 s3, s50, 131073\n"
    "s_lshl2_add_u32 s50, s3, s51\n"
    "s_lshl_b32 s50, s50, 2\n"
    "s_mov_b32 s52, 4132712976\n"
    "s_mov_b32 s53, 3989547399\n"
    "s_bitcmp1_b32 s43, 0\n"
    "s_cmov_b32 s52, 911536656\n"
    "s_cmov_b32 s53, 2915821959\n"
    "s_lshr_b64 s[52:53], s[52:53], s50\n"
    "s_and_b32 s45, s52, 15\n"
    "s_lshl1_add_u32 s50, s51, s43\n"
    "s_and_b32 s50, s50, 7\n"
    "s_lshl_b32 s50, s50, 2\n"
    "s_mov_b32 s52, 1985229328\n"
    "s_mov_b32 s53, s52\n"
    "s_lshr_b64 s[52:53], s[52:53], s50\n"
    "s_mov_b32 s49, s52\n"
    "s_and_b32 s48, s49, 7\n"
    "s_lshr_b32 s50, s40, 1\n"
    "s_cmp_eq_u32 s50, 2\n"
    "s_cbranch_scc1 .Lk_loader\n"
    ".Lk_stub:\n"
    "s_lshr_b32 s51, s2, 6\n"
    "s_and_b32 s42, s2, 7\n"
    "s_lshr_b32 s47, s2, 5\n"
    "s_cmp_eq_u32 s47, 3\n"
    "s_cselect_b32 s47, 6, s47\n"
    "s_cmp_ge_u32 s45, 14\n"
    "s_cselect_b32 s52, 3, 7\n"
    "s_cmp_eq_u32 s51, 3\n"
    "s_cselect_b32 s47, s52, s47\n"
    "s_cselect_b32 s50, 1, 0\n"
    "s_lshr_b32 s50, s43, s50\n"
    "s_and_b32 s50, s50, 3\n"
    "s_lshl2_add_u32 s46, s42, s50\n"
    "s_lshr_b32 s50, s40, 1\n"
    "s_lshl_b32 s63, s2, 2\n"
    "s_lshl1_add_u32 s51, s2, s41\n"
    "s_lshl_b32 s51, s51, 8\n"
    "v_lshl_add_u32 v8, v1, 2, s51\n"
    "s_cmp_eq_u32 s50, 1\n"
    "s_waitcnt lgkmcnt(0)\n"
    "s_cbranch_scc1 .Lk_gsetup\n"
    "global_load_dword v2, v8, s[28:29]\n"
    "global_load_dword v3, v8, s[30:31]\n"
    "global_load_dword v4, v8, s[32:33]\n"
    "global_load_dword v5, v8, s[34:35]\n"
    "s_load_dword s58, s[36:37], s63\n"
    "s_cmp_lt_u32 s40, 4\n"
    "s_cbranch_scc1 .Lk_gsetup\n"
    "buffer_load_dwordx2 v[6:7], off, s[12:15], 0 sc1\n"
    "s_load_dword s59, s[38:39], s63\n"
    "s_branch .Lk_kgen\n"
    ".Lk_loader:\n"
    "v_lshrrev_b32 v2, 4, v1\n"
    "v_xor_b32 v2, v2, v1\n"
    "v_and_b32 v2, 7, v2\n"
    "v_lshrrev_b32 v3, 3, v1\n"
    "v_lshlrev_b32 v3, 10, v3\n"
    "v_lshl_or_b32 v8, v2, 4, v3\n"
    "v_xor_b32 v2, 64, v8\n"
    "v_add_u32 v9, 7168, v2\n"
    "v_add_u32 v10, 14336, v8\n"
    "v_add_u32 v11, 21504, v2\n"
    "v_add_u32 v12, 32768, v8\n"
    "v_add_u32 v13, 39936, v2\n"
    "v_add_u32 v14, 47104, v8\n"
    "v_add_u32 v15, 54272, v2\n"
    "s_lshl1_add_u32 s60, s44, s41\n"
    "s_lshl_b32 s60, s60, 16\n"
    "s_lshl_b32 s64, s41, 13\n"
    "v_mul_u32_u24 v2, 171, v1\n"
    "v_lshlrev_b32 v3, 4, v1\n"
    "s_movk_i32 s51, 2560\n"
    "v_lshrrev_b32 v32, 11, v2\n"
    "v_mad_u32_u24 v5, v32, s51, v3\n"
    "v_add_u32 v2, 10944, v2\n"
    "v_lshrrev_b32 v33, 11, v2\n"
    "v_mad_u32_u24 v6, v33, s51, v3\n"
    "v_add_u32 v2, 10944, v2\n"
    "v_lshrrev_b32 v34, 11, v2\n"
    "v_mad_u32_u24 v7, v34, s51, v3\n"
    "s_cmp_eq_u32 s45, 14\n"
    "s_cbranch_scc0 .Lk_ct14\n"
    "v_lshl_add_u32 v2, v32, 2, v1\n"
    "v_and_b32 v2, 12, v2\n"
    "v_lshl_add_u32 v5, v2, 22, v5\n"
    "v_lshl_add_u32 v2, v33, 2, v1\n"
    "v_and_b32 v2, 12, v2\n"
    "v_lshl_add_u32 v6, v2, 22, v6\n"
    "v_lshl_add_u32 v2, v34, 2, v1\n"
    "v_and_b32 v2, 12, v2\n"
    "v_lshl_add_u32 v7, v2, 22, v7\n"
    ".Lk_ct14:\n"
    "v_add_u32 v16, 0, v5\n"
    "v_add_u32 v17, 0, v6\n"
    "v_add_u32 v18, 0, v7\n"
    "v_add_u32 v19, 40960, v5\n"
    "v_add_u32 v20, 45056, v6\n"
    "v_add_u32 v21, 45056, v7\n"
    "v_add_u32 v22, 86016, v5\n"
    "v_add_u32 v23, 86016, v6\n"
    "v_add_u32 v24, 90112, v7\n"
    "v_add_u32 v25, 131072, v5\n"
    "v_add_u32 v26, 131072, v6\n"
    "v_add_u32 v27, 131072, v7\n"
    "s_mul_i32 s61, s45, 192\n"
    "s_mul_i32 s65, s41, 12320\n"
    "s_add_u32 s65, s65, 16384\n"
    "s_mov_b32 s63, 0\n"
    "s_cmp_eq_u32 s41, 0\n"
    "s_waitcnt lgkmcnt(0)\n"
    "s_cselect_b64 s[8:9], s[8:9], s[16:17]\n"
    "s_lshl_b32 s31, s48, 7\n"
    "s_add_u32 s31, s31, s60\n"
    ".Lk_lstage:\n"
    "s_mov_b32 m0, s64\n"
    "s_mul_i32 s33, s48, 176128\n"
    "buffer_load_dwordx4 v8, s[4:7], s31 offen lds\n"
    "s_add_u32 s33, s33, s61\n"
    "buffer_load_dwordx4 v9, s[4:7], s31 offen offset:1024 lds\n"
    "s_lshr_b32 s49, s49, 4\n"
    "buffer_load_dwordx4 v10, s[4:7], s31 offen offset:2048 lds\n"
    "s_and_b32 s48, s49, 7\n"
    "buffer_load_dwordx4 v11, s[4:7], s31 offen offset:3072 lds\n"
    "s_add_u32 m0, s64, 4096\n"
    "s_cmp_ge_u32 s64, 81984\n"
    "s_cselect_b32 s34, 122976, 0\n"
    "buffer_load_dwordx4 v12, s[4:7], s31 offen lds\n"
    "s_sub_u32 s34, 40992, s34\n"
    "buffer_load_dwordx4 v13, s[4:7], s31 offen offset:1024 lds\n"
    "s_add_u32 s64, s64, s34\n"
    "s_add_u32 s63, s63, 1\n"
    "buffer_load_dwordx4 v14, s[4:7], s31 offen offset:2048 lds\n"
    "s_cmp_eq_u32 s45, 15\n"
    "buffer_load_dwordx4 v15, s[4:7], s31 offen offset:3072 lds\n"
    "s_cbranch_scc1 .Lk_lspare\n"
    "s_mov_b32 m0, s65\n"
    "s_lshl_b32 s31, s48, 7\n"
    "buffer_load_dwordx4 v16, s[8:11], s33 offen lds\n"
    "buffer_load_dwordx4 v17, s[8:11], s33 offen offset:1024 lds\n"
    "s_add_u32 s31, s31, s60\n"
    "buffer_load_dwordx4 v18, s[8:11], s33 offen offset:2048 lds\n"
    "buffer_load_dwordx4 v19, s[8:11], s33 offen offset:3072 lds\n"
    "s_cmp_lt_u32 s63, 3\n"
    "s_cbranch_scc1 .Lk_lnopf\n"
    "s_load_dword s68, s[66:67], 0\n"
    "s_load_dword s68, s[66:67], 32768\n"
    "s_load_dword s68, s[66:67], 65536\n"
    "s_load_dword s68, s[66:67], 98304\n"
    "s_load_dword s68, s[66:67], 131072\n"
    "s_load_dword s68, s[66:67], 163840\n"
    "s_load_dword s68, s[66:67], 196608\n"
    "s_add_u32 s66, s66, 229376\n"
    "s_addc_u32 s67, s67, 0\n"
    ".Lk_lnopf:\n"
    "s_add_u32 m0, s65, 4096\n"
    "s_nop 0\n"
    "buffer_load_dwordx4 v20, s[8:11], s33 offen lds\n"
    "buffer_load_dwordx4 v21, s[8:11], s33 offen offset:1024 lds\n"
    "buffer_load_dwordx4 v22, s[8:11], s33 offen offset:2048 lds\n"
    "buffer_load_dwordx4 v23, s[8:11], s33 offen offset:3072 lds\n"
    "s_add_u32 m0, s65, 8192\n"
    "s_add_u32 s65, s65, s34\n"
    "buffer_load_dwordx4 v24, s[8:11], s33 offen lds\n"
    "buffer_load_dwordx4 v25, s[8:11], s33 offen offset:1024 lds\n"
    "s_cmp_eq_u32 s63, 1\n"
    "buffer_load_dwordx4 v26, s[8:11], s33 offen offset:2048 lds\n"
    "buffer_load_dwordx4 v27, s[8:11], s33 offen offset:3072 lds\n"
    ".Lk_lcont:\n"
    "s_cbranch_scc1 .Lk_cvset\n"
    "s_waitcnt vmcnt(20)\n"
    "s_cmp_lt_u32 s63, 8\n"
    "s_barrier\n"
    "s_cbranch_scc1 .Lk_lstage\n"
    ".Lk_l1:\n"
    "s_mov_b32 m0, s29\n"
    "s_nop 0\n"
    "buffer_load_dwordx4 v28, s[24:27], s28 offen lds\n"
    "buffer_load_dwordx4 v29, s[24:27], s28 offen offset:1024 lds\n"
    "buffer_load_dwordx4 v30, s[24:27], s28 offen offset:2048 lds\n"
    "buffer_load_dwordx4 v31, s[24:27], s28 offen offset:3072 lds\n"
    "s_add_u32 s28, s28, 65536\n"
    "s_add_u32 s29, s29, 8192\n"
    "s_sub_u32 s62, s62, 1\n"
    "s_cbranch_scc0 .Lk_l1\n"
    "s_load_dword s68, s[66:67], 0\n"
    "s_waitcnt vmcnt(16)\n"
    "s_barrier\n"
    "s_setprio 3\n"
    "s_sub_u32 s62, 4, s41\n"
    "s_sub_u32 s29, s29, 114688\n"
    ".Lk_l2:\n"
    "s_mov_b32 m0, s29\n"
    "s_nop 0\n"
    "buffer_load_dwordx4 v28, s[24:27], s28 offen lds\n"
    "buffer_load_dwordx4 v29, s[24:27], s28 offen offset:1024 lds\n"
    "buffer_load_dwordx4 v30, s[24:27], s28 offen offset:2048 lds\n"
    "buffer_load_dwordx4 v31, s[24:27], s28 offen offset:3072 lds\n"
    "s_add_u32 s28, s28, 65536\n"
    "s_add_u32 s29, s29, 8192\n"
    "s_sub_u32 s62, s62, 1\n"
    "s_cbranch_scc0 .Lk_l2\n"
    "s_mov_b32 s62, 1\n"
    "s_add_u32 s29, s29, 77824\n"
    ".Lk_l3:\n"
    "s_mov_b32 m0, s29\n"
    "s_nop 0\n"
    "buffer_load_dwordx4 v28, s[24:27], s28 offen lds\n"
    "buffer_load_dwordx4 v29, s[24:27], s28 offen offset:1024 lds\n"
    "buffer_load_dwordx4 v30, s[24:27], s28 offen offset:2048 lds\n"
    "buffer_load_dwordx4 v31, s[24:27], s28 offen offset:3072 lds\n"
    "s_add_u32 s28, s28, 65536\n"
    "s_add_u32 s29, s29, 8192\n"
    "s_sub_u32 s62, s62, 1\n"
    "s_cbranch_scc0 .Lk_l3\n"
    "s_lshl_b32 s50, s41, 2\n"
    "s_add_u32 s50, s50, 163792\n"
    "v_mov_b32 v190, s50\n"
    "s_waitcnt vmcnt(0)\n"
    "v_mov_b32 v3, 9\n"
    "ds_write_b32 v190, v3\n"
    "s_branch .Lk_conv\n"
    ".Lk_lspare:\n"
    "s_lshl_b32 s31, s48, 7\n"
    "s_add_u32 s31, s31, s60\n"
    "s_cmp_lt_u32 s63, 3\n"
    "s_cbranch_scc1 .Lk_lnopf2\n"
    "s_load_dword s68, s[66:67], 0\n"
    "s_load_dword s68, s[66:67], 32768\n"
    "s_load_dword s68, s[66:67], 65536\n"
    "s_load_dword s68, s[66:67], 98304\n"
    "s_load_dword s68, s[66:67], 131072\n"
    "s_load_dword s68, s[66:67], 163840\n"
    "s_load_dword s68, s[66:67], 196608\n"
    "s_add_u32 s66, s66, 229376\n"
    "s_addc_u32 s67, s67, 0\n"
    ".Lk_lnopf2:\n"
    "s_add_u32 s50, s63, s41\n"
    "s_cmp_eq_u32 s50, 1\n"
    "s_cbranch_scc0 .Lk_lsp2\n"
    "s_load_dwordx4 s[20:23], s[0:1], 144\n"
    "v_lshlrev_b32 v2, 4, v1\n"
    "s_mov_b32 m0, 57376\n"
    "s_waitcnt lgkmcnt(0)\n"
    "buffer_load_dwordx4 v2, s[20:23], 0 offen lds\n"
    "s_branch .Lk_cvset\n"
    ".Lk_lsp2:\n"
    "s_waitcnt vmcnt(8)\n"
    "s_cmp_eq_u32 s63, 1\n"
    "s_branch .Lk_lcont\n"
    ".Lk_cvset:\n"
    "v_lshrrev_b32 v184, 4, v1\n"
    "v_and_b32 v185, 15, v1\n"
    "v_and_b32 v186, 3, v1\n"
    "v_bfe_u32 v187, v1, 2, 2\n"
    "s_mul_i32 s50, s41, 9920\n"
    "s_add_u32 s50, s50, 134720\n"
    "s_bfe_u32 s52, s40, 65537\n"
    "s_add_u32 s51, s50, 8704\n"
    "v_sub_u32 v160, v186, v184\n"
    "v_lshl_add_u32 v160, v160, 2, v187\n"
    "v_lshl_add_u32 v234, v160, 3, s51\n"
    "v_and_b32 v188, 1, v187\n"
    "v_lshl_add_u32 v188, v188, 3, s50\n"
    "v_lshrrev_b32 v187, 1, v187\n"
    "v_lshlrev_b32 v189, 1, v184\n"
    "v_sub_u32 v189, v186, v189\n"
    "v_lshl_add_u32 v189, s52, 3, v189\n"
    "v_add_u32 v160, 8, v189\n"
    "v_and_b32 v161, 1, v160\n"
    "v_lshl_or_b32 v161, v161, 1, v187\n"
    "v_bfe_u32 v162, v160, 2, 2\n"
    "v_xor_b32 v161, v161, v162\n"
    "v_lshrrev_b32 v160, 1, v160\n"
    "v_lshl_add_u32 v160, v160, 6, v188\n"
    "v_lshl_add_u32 v152, v161, 4, v160\n"
    "v_add_u32 v160, 7, v189\n"
    "v_and_b32 v161, 1, v160\n"
    "v_lshl_or_b32 v161, v161, 1, v187\n"
    "v_bfe_u32 v162, v160, 2, 2\n"
    "v_xor_b32 v161, v161, v162\n"
    "v_lshrrev_b32 v160, 1, v160\n"
    "v_lshl_add_u32 v160, v160, 6, v188\n"
    "v_lshl_add_u32 v153, v161, 4, v160\n"
    "s_lshl_b32 s51, s40, 12\n"
    "s_add_u32 s53, s51, 127040\n"
    "v_lshl_add_u32 v156, v1, 4, s53\n"
    "s_xor_b32 s51, s51, 8192\n"
    "s_add_u32 s53, s51, 127040\n"
    "v_lshl_add_u32 v157, v1, 4, s53\n"
    "s_xor_b32 s51, s40, 2\n"
    "s_lshl_b32 s51, s51, 2\n"
    "s_add_u32 s51, s51, 163808\n"
    "v_mov_b32 v158, s51\n"
    "s_lshl_b32 s51, s52, 2\n"
    "s_sub_u32 s51, 30, s51\n"
    "v_lshl_add_u32 v160, v185, 1, v184\n"
    "v_add_u32 v160, s51, v160\n"
    "v_bfe_u32 v161, v160, 3, 1\n"
    "v_xor_b32 v161, s41, v161\n"
    "v_lshl_add_u32 v161, v160, 1, v161\n"
    "s_mov_b32 s51, 153792\n"
    "v_lshl_add_u32 v154, v161, 4, s51\n"
    "v_xor_b32 v155, 16, v154\n"
    "s_xor_b32 s51, s52, 1\n"
    "s_lshl_b32 s51, s51, 11\n"
    "s_add_u32 s51, s51, 154560\n"
    "v_lshl_add_u32 v160, v185, 1, v184\n"
    "v_add_u32 v160, 29, v160\n"
    "v_bfe_u32 v161, v160, 3, 1\n"
    "v_xor_b32 v161, s41, v161\n"
    "v_lshl_add_u32 v161, v160, 1, v161\n"
    "v_lshl_add_u32 v232, v161, 4, s51\n"
    "v_lshl_add_u32 v160, v185, 1, v184\n"
    "v_add_u32 v160, 30, v160\n"
    "v_bfe_u32 v161, v160, 3, 1\n"
    "v_xor_b32 v161, s41, v161\n"
    "v_lshl_add_u32 v161, v160, 1, v161\n"
    "v_lshl_add_u32 v233, v161, 4, s51\n"
    "v_add_u32 v235, 8192, v152\n"
    "v_add_u32 v236, 8192, v153\n"
    "s_cmp_eq_u32 s52, 0\n"
    "s_cbranch_scc1 .Lk_f2\n"
    "v_add_u32 v235, 256, v234\n"
    "v_add_u32 v236, 928, v234\n"
    ".Lk_f2:\n"
    "v_lshlrev_b32 v161, 3, v184\n"
    "v_mov_b32 v160, 8176\n"
    "v_lshlrev_b32 v160, v185, v160\n"
    "v_lshrrev_b32 v160, v161, v160\n"
    "v_and_b32 v160, 255, v160\n"
    "v_lshl_or_b32 v160, v160, 15, v160\n"
    "v_lshrrev_b32 v162, 0, v160\n"
    "v_and_b32 v162, 65537, v162\n"
    "v_mul_u32_u24 v224, 16256, v162\n"
    "v_lshrrev_b32 v162, 2, v160\n"
    "v_and_b32 v162, 65537, v162\n"
    "v_mul_u32_u24 v225, 16256, v162\n"
    "v_lshrrev_b32 v162, 4, v160\n"
    "v_and_b32 v162, 65537, v162\n"
    "v_mul_u32_u24 v226, 16256, v162\n"
    "v_lshrrev_b32 v162, 6, v160\n"
    "v_and_b32 v162, 65537, v162\n"
    "v_mul_u32_u24 v227, 16256, v162\n"
    "v_mov_b32 v160, 4064\n"
    "v_lshlrev_b32 v160, v185, v160\n"
    "v_lshrrev_b32 v160, v161, v160\n"
    "v_and_b32 v160, 255, v160\n"
    "v_lshl_or_b32 v160, v160, 15, v160\n"
    "v_lshrrev_b32 v162, 0, v160\n"
    "v_and_b32 v162, 65537, v162\n"
    "v_mul_u32_u24 v228, 16256, v162\n"
    "v_lshrrev_b32 v162, 2, v160\n"
    "v_and_b32 v162, 65537, v162\n"
    "v_mul_u32_u24 v229, 16256, v162\n"
    "v_lshrrev_b32 v162, 4, v160\n"
    "v_and_b32 v162, 65537, v162\n"
    "v_mul_u32_u24 v230, 16256, v162\n"
    "v_lshrrev_b32 v162, 6, v160\n"
    "v_and_b32 v162, 65537, v162\n"
    "v_mul_u32_u24 v231, 16256, v162\n"
    "v_and_b32 v160, 1, v184\n"
    "v_mul_u32_u24 v160, 3, v160\n"
    "v_xor_b32 v160, s52, v160\n"
    "v_lshrrev_b32 v161, 2, v185\n"
    "v_lshl_add_u32 v160, v160, 2, v161\n"
    "v_lshrrev_b32 v161, 1, v184\n"
    "v_lshl_add_u32 v160, v160, 1, v161\n"
    "v_lshl_add_u32 v160, v160, 2, v186\n"
    "v_lshlrev_b32 v159, 4, v160\n"
    "v_and_b32 v160, 1, v1\n"
    "v_lshlrev_b32 v160, 2, v160\n"
    "v_add_u32 v163, 163800, v160\n"
    "v_lshlrev_b32 v160, 2, v186\n"
    "v_add_u32 v164, 163808, v160\n"
    "v_add_u32 v191, 163824, v160\n"
    "s_lshl_b32 s50, s40, 2\n"
    "s_add_u32 s50, s50, 163808\n"
    "v_mov_b32 v165, s50\n"
    "s_lshl1_add_u32 s50, s2, s41\n"
    "s_lshl_b32 s50, s50, 12\n"
    "s_add_u32 s30, s50, 524288\n"
    "v_mov_b32 v160, 163776\n"
    "s_cmp_ge_u32 s40, 6\n"
    "s_cbranch_scc1 .Lk_cvret\n"
    "s_lshr_b32 s51, s2, 6\n"
    "s_and_b32 s42, s2, 7\n"
    "s_lshr_b32 s47, s2, 5\n"
    "s_cmp_eq_u32 s47, 3\n"
    "s_cselect_b32 s47, 6, s47\n"
    "s_cmp_ge_u32 s45, 14\n"
    "s_cselect_b32 s52, 3, 7\n"
    "s_cmp_eq_u32 s51, 3\n"
    "s_cselect_b32 s47, s52, s47\n"
    "s_cselect_b32 s50, 1, 0\n"
    "s_lshr_b32 s50, s43, s50\n"
    "s_and_b32 s50, s50, 3\n"
    "s_lshl2_add_u32 s46, s42, s50\n"
    "s_lshl1_add_u32 s52, s2, s41\n"
    "s_lshl_b32 s52, s52, 6\n"
    "s_add_u32 s66, s24, s52\n"
    "s_addc_u32 s67, s25, 0\n"
    "v_lshrrev_b32 v29, 2, v1\n"
    "v_and_b32 v29, 4, v29\n"
    "v_and_b32 v30, 7, v1\n"
    "v_xor_b32 v29, v29, v30\n"
    "v_lshrrev_b32 v30, 3, v1\n"
    "v_lshlrev_b32 v30, 10, v30\n"
    "v_lshl_or_b32 v28, v29, 4, v30\n"
    "v_add_u32 v29, 7168, v28\n"
    "v_add_u32 v30, 14336, v28\n"
    "v_add_u32 v31, 21504, v28\n"
    "s_lshl_b32 s28, s41, 8\n"
    "s_add_u32 s28, s28, s47\n"
    "s_lshl_b32 s28, s28, 7\n"
    "s_lshl_b32 s29, s41, 12\n"
    "s_add_u32 s29, s29, 81984\n"
    "s_mov_b32 s62, 3\n"
    "s_branch .Lk_lstage\n"
    ".Lk_gsetup:\n"
    "s_load_dwordx4 s[16:19], s[0:1], 128\n"
    "v_mov_b32 v8, 0\n"
    "v_mov_b32 v9, 0\n"
    "v_mov_b32 v10, 0\n"
    "v_mov_b32 v11, 0\n"
    "v_and_b32 v12, 31, v1\n"
    "v_lshrrev_b32 v13, 5, v1\n"
    "v_mfma_f32_32x32x16_bf16 v[128:143], v[8:11], v[8:11], 0\n"
    "v_mfma_f32_32x32x16_bf16 v[144:159], v[8:11], v[8:11], 0\n"
    "v_mfma_f32_32x32x16_bf16 v[160:175], v[8:11], v[8:11], 0\n"
    "v_mfma_f32_32x32x16_bf16 v[176:191], v[8:11], v[8:11], 0\n"
    "v_mfma_f32_32x32x16_bf16 v[192:207], v[8:11], v[8:11], 0\n"
    "v_mfma_f32_32x32x16_bf16 v[208:223], v[8:11], v[8:11], 0\n"
    "v_bfe_u32 v14, v1, 1, 3\n"
    "v_xor_b32 v14, v14, v13\n"
    "v_lshlrev_b32 v14, 4, v14\n"
    "s_cmp_eq_u32 s45, 15\n"
    "s_cselect_b32 s50, s40, s41\n"
    "s_cselect_b32 s51, 12, 13\n"
    "s_lshl_b32 s50, s50, s51\n"
    "v_lshl_add_u32 v15, v12, 7, s50\n"
    "v_or_b32 v224, v14, v15\n"
    "v_xor_b32 v225, 32, v224\n"
    "v_xor_b32 v226, 64, v224\n"
    "v_xor_b32 v227, 96, v224\n"
    "s_lshr_b32 s52, s40, 1\n"
    "v_bfe_u32 v14, v1, 2, 2\n"
    "v_lshl_add_u32 v14, v13, 3, v14\n"
    "v_mul_u32_u24 v14, 192, v14\n"
    "v_bfe_u32 v15, v1, 4, 1\n"
    "v_mul_u32_u24 v15, 12320, v15\n"
    "s_mul_i32 s50, s52, 96\n"
    "s_add_u32 s50, s50, 16384\n"
    "v_add3_u32 v14, v14, v15, s50\n"
    "v_and_b32 v15, 1, v1\n"
    "v_lshl_add_u32 v14, v15, 4, v14\n"
    "v_and_b32 v15, 2, v1\n"
    "v_lshl_add_u32 v228, v15, 2, v14\n"
    "v_bfe_u32 v14, v1, 2, 2\n"
    "v_lshrrev_b32 v15, 5, v1\n"
    "v_lshl_add_u32 v15, v15, 3, v14\n"
    "s_lshl_b32 s50, s52, 11\n"
    "s_add_u32 s50, s50, 64\n"
    "v_lshlrev_b32 v15, 7, v15\n"
    "v_and_b32 v238, 16, v1\n"
    "v_add3_u32 v15, v15, v238, s50\n"
    "v_and_b32 v238, 1, v1\n"
    "v_lshl_add_u32 v15, v238, 5, v15\n"
    "v_and_b32 v238, 2, v1\n"
    "v_lshl_add_u32 v15, v238, 2, v15\n"
    "s_lshl_b32 s50, s52, 3\n"
    "v_xor_b32 v238, s50, v1\n"
    "v_and_b32 v238, 8, v238\n"
    "v_lshlrev_b32 v238, 3, v238\n"
    "v_add_u32 v230, v15, v238\n"
    "v_xad_u32 v231, v238, 64, v15\n"
    "v_and_b32 v238, 7, v1\n"
    "v_lshlrev_b32 v238, 2, v238\n"
    "v_mov_b32 v244, 2290614425\n"
    "v_lshrrev_b32 v244, v238, v244\n"
    "v_and_b32 v244, 15, v244\n"
    "v_add_u32 v238, 163792, v238\n"
    "s_add_u32 s50, s40, 21\n"
    "s_lshl_b32 s28, s50, 8\n"
    "s_add_u32 s28, s28, s47\n"
    "s_lshl_b32 s28, s28, 7\n"
    "s_lshl_b32 s29, s40, 12\n"
    "s_add_u32 s29, s29, 36928\n"
    "v_add_u32 v232, 81920, v230\n"
    "v_add_u32 v233, 81920, v231\n"
    "v_lshrrev_b32 v235, 2, v1\n"
    "v_and_b32 v235, 4, v235\n"
    "v_and_b32 v236, 7, v1\n"
    "v_xor_b32 v235, v235, v236\n"
    "v_lshrrev_b32 v236, 3, v1\n"
    "v_lshlrev_b32 v236, 10, v236\n"
    "v_lshl_or_b32 v234, v235, 4, v236\n"
    "v_add_u32 v235, 7168, v234\n"
    "v_add_u32 v236, 14336, v234\n"
    "v_add_u32 v237, 21504, v234\n"
    "s_lshl_b32 s50, s40, 2\n"
    "s_add_u32 s50, s50, 163808\n"
    "v_mov_b32 v239, s50\n"
    "v_lshlrev_b32 v240, 4, v1\n"
    "v_add_u32 v241, 88064, v240\n"
    "v_mov_b32 v242, 0xc0135761\n"
    "s_setprio 1\n"
    "s_mul_i32 s50, s45, 6\n"
    "s_mul_i32 s51, s52, 3\n"
    "s_add_u32 s50, s50, s51\n"
    "s_lshl1_add_u32 s51, s44, s41\n"
    "s_and_b32 s53, s51, 3\n"
    "s_mul_i32 s53, s53, 0\n"
    "s_mul_i32 s54, s51, 172\n"
    "s_sub_u32 s55, s50, s53\n"
    "s_cselect_b32 s56, 86, 0\n"
    "s_add_u32 s55, s55, s56\n"
    "s_cmp_lt_u32 s50, 86\n"
    "s_cselect_b32 s56, s54, 5504\n"
    "s_add_u32 s55, s55, s56\n"
    "s_lshl_b32 s55, s55, 10\n"
    "s_add_u32 s60, s55, 2621440\n"
    "s_add_u32 s50, s50, 1\n"
    "s_sub_u32 s55, s50, s53\n"
    "s_cselect_b32 s56, 86, 0\n"
    "s_add_u32 s55, s55, s56\n"
    "s_cmp_lt_u32 s50, 86\n"
    "s_cselect_b32 s56, s54, 5504\n"
    "s_add_u32 s55, s55, s56\n"
    "s_lshl_b32 s55, s55, 10\n"
    "s_add_u32 s61, s55, 2621440\n"
    "s_add_u32 s50, s50, 1\n"
    "s_sub_u32 s55, s50, s53\n"
    "s_cselect_b32 s56, 86, 0\n"
    "s_add_u32 s55, s55, s56\n"
    "s_cmp_lt_u32 s50, 86\n"
    "s_cselect_b32 s56, s54, 5504\n"
    "s_add_u32 s55, s55, s56\n"
    "s_lshl_b32 s55, s55, 10\n"
    "s_add_u32 s62, s55, 2621440\n"
    "s_cmp_eq_u32 s45, 15\n"
    "s_cselect_b32 s50, s52, s41\n"
    "s_cselect_b32 s51, s41, s52\n"
    "s_lshl1_add_u32 s50, s44, s50\n"
    "s_lshl1_add_u32 s51, s45, s51\n"
    "s_lshl_b32 s50, s50, 6\n"
    "s_add_u32 s50, s50, s51\n"
    "s_lshl_b32 s50, s50, 2\n"
    "s_add_u32 s65, s50, 81920\n"
    "s_cmp_ge_u32 s40, 2\n"
    "s_cbranch_scc1 .Lk_gmain\n"
    ".Lk_kgen:\n"
    "s_cmp_lt_u32 s40, 2\n"
    "s_cselect_b32 s50, 0, 40992\n"
    "s_cselect_b32 s54, 0, 16\n"
    "s_add_u32 s50, s50, 81984\n"
    "s_mul_i32 s51, s41, 16512\n"
    "s_add_u32 s50, s50, s51\n"
    "v_lshl_add_u32 v40, v1, 2, s50\n"
    "v_add_u32 v41, 1088, v40\n"
    "v_add_u32 v42, 2176, v40\n"
    "v_add_u32 v43, 3264, v40\n"
    "v_add_u32 v44, 4352, v40\n"
    "v_add_u32 v45, 5440, v40\n"
    "v_add_u32 v46, 6528, v40\n"
    "v_add_u32 v47, 7616, v40\n"
    "v_add_u32 v48, 8704, v40\n"
    "v_add_u32 v49, 9792, v40\n"
    "v_add_u32 v50, 10880, v40\n"
    "v_add_u32 v51, 11968, v40\n"
    "v_add_u32 v52, 13056, v40\n"
    "v_add_u32 v53, 14144, v40\n"
    "v_and_b32 v12, 31, v1\n"
    "v_lshrrev_b32 v13, 5, v1\n"
    "v_lshl_add_u32 v13, v13, 4, s50\n"
    "s_movk_i32 s51, 272\n"
    "v_mad_u32_u24 v57, v12, s51, v13\n"
    "v_mov_b32 v14, 0x3020100\n"
    "s_lshl_b32 exec_lo, exec_lo, 4\n"
    "s_mov_b32 exec_hi, exec_lo\n"
    "v_mov_b32 v14, 0xa090808\n"
    "s_lshl_b32 exec_lo, exec_lo, 4\n"
    "s_mov_b32 exec_hi, exec_lo\n"
    "v_mov_b32 v14, 0x7060504\n"
    "s_lshl_b32 exec_lo, exec_lo, 4\n"
    "s_mov_b32 exec_hi, exec_lo\n"
    "v_mov_b32 v14, 0xc0b0a0a\n"
    "s_lshl_b32 exec_lo, exec_lo, 4\n"
    "s_mov_b32 exec_hi, exec_lo\n"
    "v_mov_b32 v14, 0x130f0e0d\n"
    "s_lshl_b32 exec_lo, exec_lo, 4\n"
    "s_mov_b32 exec_hi, exec_lo\n"
    "v_mov_b32 v14, 0x14121110\n"
    "s_lshl_b32 exec_lo, exec_lo, 4\n"
    "s_mov_b32 exec_hi, exec_lo\n"
    "v_mov_b32 v14, 0x15\n"
    "s_lshl_b32 exec_lo, exec_lo, 4\n"
    "s_mov_b32 exec_hi, exec_lo\n"
    "v_mov_b32 v14, 0x16\n"
    "s_mov_b64 exec, -1\n"
    "v_lshlrev_b32 v15, 3, v1\n"
    "v_lshrrev_b32 v14, v15, v14\n"
    "v_and_b32 v14, 31, v14\n"
    "v_add_u32 v14, 32, v14\n"
    "v_mad_u32_u24 v56, v14, s51, v13\n"
    "s_lshl_b32 s55, s41, 4\n"
    "s_xor_b32 s55, s55, 16\n"
    "s_waitcnt lgkmcnt(0)\n"
    "s_lshl_b32 s55, s58, s55\n"
    "s_and_b32 s55, s55, 4294901760\n"
    "v_mov_b32 v8, 0x3fb8aa3b\n"
    "v_mul_f32 v9, s55, v8\n"
    "v_exp_f32 v9, v9\n"
    "s_waitcnt vmcnt(3)\n"
    "v_lshlrev_b32 v10, s54, v2\n"
    "v_and_b32 v10, 4294901760, v10\n"
    "v_mul_f32 v10, v10, v8\n"
    "v_cvt_pk_bf16_f32 v9, v9, v9\n"
    "v_exp_f32 v10, v10\n"
    "v_lshlrev_b32 v9, 16, v9\n"
    "s_waitcnt vmcnt(2)\n"
    "v_lshlrev_b32 v11, s54, v3\n"
    "v_and_b32 v11, 4294901760, v11\n"
    "v_cvt_pk_bf16_f32 v10, v10, v10\n"
    "v_mul_f32 v11, v11, v9\n"
    "v_lshlrev_b32 v10, 16, v10\n"
    "v_mul_f32 v11, 0.15915494, v11\n"
    "v_mul_f32_e64 v10, -v10, v9\n"
    "v_mul_f32 v10, v10, v8\n"
    "v_mul_f32 v12, 0x41000000, v10\n"
    "v_mul_f32 v13, 0x41000000, v11\n"
    "v_mul_f32 v14, 0x42000000, v10\n"
    "v_mul_f32 v15, 0x42000000, v11\n"
    "v_exp_f32 v12, v12\n"
    "v_fract_f32 v13, v13\n"
    "v_exp_f32 v14, v14\n"
    "v_fract_f32 v15, v15\n"
    "v_cos_f32 v18, v13\n"
    "v_sin_f32 v19, v13\n"
    "v_cos_f32 v24, v15\n"
    "v_sin_f32 v25, v15\n"
    "v_pk_mul_f32 v[18:19], v[18:19], v[12:13] op_sel_hi:[1,0]\n"
    "v_pk_mul_f32 v[24:25], v[24:25], v[14:15] op_sel_hi:[1,0]\n"
    "v_pk_mul_f32 v[58:59], v[18:19], v[18:19] op_sel_hi:[0,1]\n"
    "s_nop 0\n"
    "v_pk_fma_f32 v[20:21], v[18:19], v[18:19], v[58:59] op_sel:[1,1,0] op_sel_hi:[1,0,1] neg_lo:[0,1,0]\n"
    "s_nop 0\n"
    "v_pk_mul_f32 v[58:59], v[20:21], v[18:19] op_sel_hi:[0,1]\n"
    "s_nop 0\n"
    "v_pk_fma_f32 v[22:23], v[20:21], v[18:19], v[58:59] op_sel:[1,1,0] op_sel_hi:[1,0,1] neg_lo:[0,1,0]\n"
    "v_mov_b32 v36, 16256\n"
    "v_cvt_pk_bf16_f32 v37, v18, v19\n"
    "v_cvt_pk_bf16_f32 v38, v20, v21\n"
    "v_cvt_pk_bf16_f32 v39, v22, v23\n"
    "v_pk_mul_f32 v[58:59], v[18:19], v[24:25] op_sel_hi:[0,1]\n"
    "v_pk_mul_f32 v[60:61], v[20:21], v[24:25] op_sel_hi:[0,1]\n"
    "v_pk_mul_f32 v[62:63], v[22:23], v[24:25] op_sel_hi:[0,1]\n"
    "ds_write2_b32 v40, v36, v37 offset0:0 offset1:68\n"
    "ds_write2_b32 v40, v38, v39 offset0:136 offset1:204\n"
    "v_pk_fma_f32 v[18:19], v[18:19], v[24:25], v[58:59] op_sel:[1,1,0] op_sel_hi:[1,0,1] neg_lo:[0,1,0]\n"
    "v_pk_fma_f32 v[20:21], v[20:21], v[24:25], v[60:61] op_sel:[1,1,0] op_sel_hi:[1,0,1] neg_lo:[0,1,0]\n"
    "v_pk_fma_f32 v[22:23], v[22:23], v[24:25], v[62:63] op_sel:[1,1,0] op_sel_hi:[1,0,1] neg_lo:[0,1,0]\n"
    "v_cvt_pk_bf16_f32 v36, v24, v25\n"
    "v_cvt_pk_bf16_f32 v37, v18, v19\n"
    "v_cvt_pk_bf16_f32 v38, v20, v21\n"
    "v_cvt_pk_bf16_f32 v39, v22, v23\n"
    "v_pk_mul_f32 v[58:59], v[24:25], v[24:25] op_sel_hi:[0,1]\n"
    "v_pk_mul_f32 v[60:61], v[18:19], v[24:25] op_sel_hi:[0,1]\n"
    "v_pk_mul_f32 v[62:63], v[20:21], v[24:25] op_sel_hi:[0,1]\n"
    "v_pk_mul_f32 v[12:13], v[22:23], v[24:25] op_sel_hi:[0,1]\n"
    "ds_write2_b32 v41, v36, v37 offset0:0 offset1:68\n"
    "ds_write2_b32 v41, v38, v39 offset0:136 offset1:204\n"
    "v_pk_fma_f32 v[16:17], v[24:25], v[24:25], v[58:59] op_sel:[1,1,0] op_sel_hi:[1,0,1] neg_lo:[0,1,0]\n"
    "v_pk_fma_f32 v[18:19], v[18:19], v[24:25], v[60:61] op_sel:[1,1,0] op_sel_hi:[1,0,1] neg_lo:[0,1,0]\n"
    "v_pk_fma_f32 v[20:21], v[20:21], v[24:25], v[62:63] op_sel:[1,1,0] op_sel_hi:[1,0,1] neg_lo:[0,1,0]\n"
    "v_pk_fma_f32 v[22:23], v[22:23], v[24:25], v[12:13] op_sel:[1,1,0] op_sel_hi:[1,0,1] neg_lo:[0,1,0]\n"
    "v_cvt_pk_bf16_f32 v36, v16, v17\n"
    "v_cvt_pk_bf16_f32 v37, v18, v19\n"
    "v_cvt_pk_bf16_f32 v38, v20, v21\n"
    "v_cvt_pk_bf16_f32 v39, v22, v23\n"
    "v_pk_mul_f32 v[58:59], v[16:17], v[24:25] op_sel_hi:[0,1]\n"
    "v_pk_mul_f32 v[60:61], v[18:19], v[24:25] op_sel_hi:[0,1]\n"
    "v_pk_mul_f32 v[62:63], v[20:21], v[24:25] op_sel_hi:[0,1]\n"
    "v_pk_mul_f32 v[12:13], v[22:23], v[24:25] op_sel_hi:[0,1]\n"
    "ds_write2_b32 v42, v36, v37 offset0:0 offset1:68\n"
    "ds_write2_b32 v42, v38, v39 offset0:136 offset1:204\n"
    "v_pk_fma_f32 v[16:17], v[16:17], v[24:25], v[58:59] op_sel:[1,1,0] op_sel_hi:[1,0,1] neg_lo:[0,1,0]\n"
    "v_pk_fma_f32 v[18:19], v[18:19], v[24:25], v[60:61] op_sel:[1,1,0] op_sel_hi:[1,0,1] neg_lo:[0,1,0]\n"
    "v_pk_fma_f32 v[20:21], v[20:21], v[24:25], v[62:63] op_sel:[1,1,0] op_sel_hi:[1,0,1] neg_lo:[0,1,0]\n"
    "v_pk_fma_f32 v[22:23], v[22:23], v[24:25], v[12:13] op_sel:[1,1,0] op_sel_hi:[1,0,1] neg_lo:[0,1,0]\n"
    "v_cvt_pk_bf16_f32 v36, v16, v17\n"
    "v_cvt_pk_bf16_f32 v37, v18, v19\n"
    "v_cvt_pk_bf16_f32 v38, v20, v21\n"
    "v_cvt_pk_bf16_f32 v39, v22, v23\n"
    "v_pk_mul_f32 v[58:59], v[16:17], v[24:25] op_sel_hi:[0,1]\n"
    "v_pk_mul_f32 v[60:61], v[18:19], v[24:25] op_sel_hi:[0,1]\n"
    "v_pk_mul_f32 v[62:63], v[20:21], v[24:25] op_sel_hi:[0,1]\n"
    "v_pk_mul_f32 v[12:13], v[22:23], v[24:25] op_sel_hi:[0,1]\n"
    "ds_write2_b32 v43, v36, v37 offset0:0 offset1:68\n"
    "ds_write2_b32 v43, v38, v39 offset0:136 offset1:204\n"
    "v_pk_fma_f32 v[16:17], v[16:17], v[24:25], v[58:59] op_sel:[1,1,0] op_sel_hi:[1,0,1] neg_lo:[0,1,0]\n"
    "v_pk_fma_f32 v[18:19], v[18:19], v[24:25], v[60:61] op_sel:[1,1,0] op_sel_hi:[1,0,1] neg_lo:[0,1,0]\n"
    "v_pk_fma_f32 v[20:21], v[20:21], v[24:25], v[62:63] op_sel:[1,1,0] op_sel_hi:[1,0,1] neg_lo:[0,1,0]\n"
    "v_pk_fma_f32 v[22:23], v[22:23], v[24:25], v[12:13] op_sel:[1,1,0] op_sel_hi:[1,0,1] neg_lo:[0,1,0]\n"
    "v_cvt_pk_bf16_f32 v36, v16, v17\n"
    "v_cvt_pk_bf16_f32 v37, v18, v19\n"
    "v_cvt_pk_bf16_f32 v38, v20, v21\n"
    "v_cvt_pk_bf16_f32 v39, v22, v23\n"
    "v_pk_mul_f32 v[58:59], v[16:17], v[24:25] op_sel_hi:[0,1]\n"
    "v_pk_mul_f32 v[60:61], v[18:19], v[24:25] op_sel_hi:[0,1]\n"
    "v_pk_mul_f32 v[62:63], v[20:21], v[24:25] op_sel_hi:[0,1]\n"
    "v_pk_mul_f32 v[12:13], v[22:23], v[24:25] op_sel_hi:[0,1]\n"
    "ds_write2_b32 v44, v36, v37 offset0:0 offset1:68\n"
    "ds_write2_b32 v44, v38, v39 offset0:136 offset1:204\n"
    "v_pk_fma_f32 v[16:17], v[16:17], v[24:25], v[58:59] op_sel:[1,1,0] op_sel_hi:[1,0,1] neg_lo:[0,1,0]\n"
    "v_pk_fma_f32 v[18:19], v[18:19], v[24:25], v[60:61] op_sel:[1,1,0] op_sel_hi:[1,0,1] neg_lo:[0,1,0]\n"
    "v_pk_fma_f32 v[20:21], v[20:21], v[24:25], v[62:63] op_sel:[1,1,0] op_sel_hi:[1,0,1] neg_lo:[0,1,0]\n"
    "v_pk_fma_f32 v[22:23], v[22:23], v[24:25], v[12:13] op_sel:[1,1,0] op_sel_hi:[1,0,1] neg_lo:[0,1,0]\n"
    "v_cvt_pk_bf16_f32 v36, v16, v17\n"
    "v_cvt_pk_bf16_f32 v37, v18, v19\n"
    "v_cvt_pk_bf16_f32 v38, v20, v21\n"
    "v_cvt_pk_bf16_f32 v39, v22, v23\n"
    "v_pk_mul_f32 v[58:59], v[16:17], v[24:25] op_sel_hi:[0,1]\n"
    "v_pk_mul_f32 v[60:61], v[18:19], v[24:25] op_sel_hi:[0,1]\n"
    "v_pk_mul_f32 v[62:63], v[20:21], v[24:25] op_sel_hi:[0,1]\n"
    "v_pk_mul_f32 v[12:13], v[22:23], v[24:25] op_sel_hi:[0,1]\n"
    "ds_write2_b32 v45, v36, v37 offset0:0 offset1:68\n"
    "ds_write2_b32 v45, v38, v39 offset0:136 offset1:204\n"
    "v_pk_fma_f32 v[16:17], v[16:17], v[24:25], v[58:59] op_sel:[1,1,0] op_sel_hi:[1,0,1] neg_lo:[0,1,0]\n"
    "v_pk_fma_f32 v[18:19], v[18:19], v[24:25], v[60:61] op_sel:[1,1,0] op_sel_hi:[1,0,1] neg_lo:[0,1,0]\n"
    "v_pk_fma_f32 v[20:21], v[20:21], v[24:25], v[62:63] op_sel:[1,1,0] op_sel_hi:[1,0,1] neg_lo:[0,1,0]\n"
    "v_pk_fma_f32 v[22:23], v[22:23], v[24:25], v[12:13] op_sel:[1,1,0] op_sel_hi:[1,0,1] neg_lo:[0,1,0]\n"
    "v_cvt_pk_bf16_f32 v36, v16, v17\n"
    "v_cvt_pk_bf16_f32 v37, v18, v19\n"
    "v_cvt_pk_bf16_f32 v38, v20, v21\n"
    "v_cvt_pk_bf16_f32 v39, v22, v23\n"
    "v_pk_mul_f32 v[58:59], v[16:17], v[24:25] op_sel_hi:[0,1]\n"
    "v_pk_mul_f32 v[60:61], v[18:19], v[24:25] op_sel_hi:[0,1]\n"
    "v_pk_mul_f32 v[62:63], v[20:21], v[24:25] op_sel_hi:[0,1]\n"
    "v_pk_mul_f32 v[12:13], v[22:23], v[24:25] op_sel_hi:[0,1]\n"
    "ds_write2_b32 v46, v36, v37 offset0:0 offset1:68\n"
    "ds_write2_b32 v46, v38, v39 offset0:136 offset1:204\n"
    "v_pk_fma_f32 v[16:17], v[16:17], v[24:25], v[58:59] op_sel:[1,1,0] op_sel_hi:[1,0,1] neg_lo:[0,1,0]\n"
    "v_pk_fma_f32 v[18:19], v[18:19], v[24:25], v[60:61] op_sel:[1,1,0] op_sel_hi:[1,0,1] neg_lo:[0,1,0]\n"
    "v_pk_fma_f32 v[20:21], v[20:21], v[24:25], v[62:63] op_sel:[1,1,0] op_sel_hi:[1,0,1] neg_lo:[0,1,0]\n"
    "v_pk_fma_f32 v[22:23], v[22:23], v[24:25], v[12:13] op_sel:[1,1,0] op_sel_hi:[1,0,1] neg_lo:[0,1,0]\n"
    "v_cvt_pk_bf16_f32 v36, v16, v17\n"
    "v_cvt_pk_bf16_f32 v37, v18, v19\n"
    "v_cvt_pk_bf16_f32 v38, v20, v21\n"
    "v_cvt_pk_bf16_f32 v39, v22, v23\n"
    "ds_write2_b32 v47, v36, v37 offset0:0 offset1:68\n"
    "ds_write2_b32 v47, v38, v39 offset0:136 offset1:204\n"
    "ds_read_b128 v[64:67], v57\n"
    "ds_read_b128 v[68:71], v57 offset:32\n"
    "ds_read_b128 v[72:75], v57 offset:64\n"
    "ds_read_b128 v[76:79], v57 offset:96\n"
    "ds_read_b128 v[80:83], v57 offset:128\n"
    "ds_read_b128 v[84:87], v57 offset:160\n"
    "ds_read_b128 v[88:91], v57 offset:192\n"
    "ds_read_b128 v[92:95], v57 offset:224\n"
    "v_mul_f32 v12, 0x3f800000, v10\n"
    "v_mul_f32 v13, 0xbf800000, v11\n"
    "v_mul_f32 v14, 0x43800000, v10\n"
    "v_mul_f32 v15, 0xc3800000, v11\n"
    "v_exp_f32 v12, v12\n"
    "v_fract_f32 v13, v13\n"
    "v_exp_f32 v14, v14\n"
    "v_fract_f32 v15, v15\n"
    "v_cos_f32 v34, v13\n"
    "v_sin_f32 v35, v13\n"
    "v_cos_f32 v96, v15\n"
    "v_sin_f32 v97, v15\n"
    "v_pk_mul_f32 v[34:35], v[34:35], v[12:13] op_sel_hi:[1,0]\n"
    "v_pk_mul_f32 v[96:97], v[96:97], v[14:15] op_sel_hi:[1,0]\n"
    "s_waitcnt vmcnt(1)\n"
    "v_lshlrev_b32 v16, s54, v4\n"
    "v_and_b32 v16, 4294901760, v16\n"
    "s_waitcnt vmcnt(0)\n"
    "v_lshlrev_b32 v17, s54, v5\n"
    "v_and_b32 v17, 4294901760, v17\n"
    "v_mul_f32 v9, 0x20000000, v9\n"
    "v_mul_f32 v16, v16, v9\n"
    "v_mul_f32_e64 v17, -v17, v9\n"
    "v_pk_mul_f32 v[58:59], v[34:35], v[34:35] op_sel_hi:[0,1]\n"
    "v_pk_mul_f32 v[60:61], v[16:17], v[34:35] op_sel_hi:[0,1]\n"
    "v_pk_fma_f32 v[98:99], v[34:35], v[34:35], v[58:59] op_sel:[1,1,0] op_sel_hi:[1,0,1] neg_lo:[0,1,0]\n"
    "v_pk_fma_f32 v[18:19], v[16:17], v[34:35], v[60:61] op_sel:[1,1,0] op_sel_hi:[1,0,1] neg_lo:[0,1,0]\n"
    "v_pk_mul_f32 v[58:59], v[98:99], v[98:99] op_sel_hi:[0,1]\n"
    "v_pk_mul_f32 v[60:61], v[16:17], v[98:99] op_sel_hi:[0,1]\n"
    "v_pk_mul_f32 v[62:63], v[18:19], v[98:99] op_sel_hi:[0,1]\n"
    "v_pk_fma_f32 v[100:101], v[98:99], v[98:99], v[58:59] op_sel:[1,1,0] op_sel_hi:[1,0,1] neg_lo:[0,1,0]\n"
    "v_pk_fma_f32 v[20:21], v[16:17], v[98:99], v[60:61] op_sel:[1,1,0] op_sel_hi:[1,0,1] neg_lo:[0,1,0]\n"
    "v_pk_fma_f32 v[22:23], v[18:19], v[98:99], v[62:63] op_sel:[1,1,0] op_sel_hi:[1,0,1] neg_lo:[0,1,0]\n"
    "v_cvt_pk_bf16_f32 v36, v16, v17\n"
    "v_cvt_pk_bf16_f32 v37, v18, v19\n"
    "v_cvt_pk_bf16_f32 v38, v20, v21\n"
    "v_cvt_pk_bf16_f32 v39, v22, v23\n"
    "v_pk_mul_f32 v[58:59], v[16:17], v[100:101] op_sel_hi:[0,1]\n"
    "v_pk_mul_f32 v[60:61], v[18:19], v[100:101] op_sel_hi:[0,1]\n"
    "v_pk_mul_f32 v[62:63], v[20:21], v[100:101] op_sel_hi:[0,1]\n"
    "v_pk_mul_f32 v[12:13], v[22:23], v[100:101] op_sel_hi:[0,1]\n"
    "ds_write2_b32 v48, v36, v37 offset0:0 offset1:68\n"
    "ds_write2_b32 v48, v38, v39 offset0:136 offset1:204\n"
    "v_pk_fma_f32 v[24:25], v[16:17], v[100:101], v[58:59] op_sel:[1,1,0] op_sel_hi:[1,0,1] neg_lo:[0,1,0]\n"
    "v_pk_fma_f32 v[26:27], v[18:19], v[100:101], v[60:61] op_sel:[1,1,0] op_sel_hi:[1,0,1] neg_lo:[0,1,0]\n"
    "v_pk_fma_f32 v[28:29], v[20:21], v[100:101], v[62:63] op_sel:[1,1,0] op_sel_hi:[1,0,1] neg_lo:[0,1,0]\n"
    "v_pk_fma_f32 v[30:31], v[22:23], v[100:101], v[12:13] op_sel:[1,1,0] op_sel_hi:[1,0,1] neg_lo:[0,1,0]\n"
    "v_cvt_pk_bf16_f32 v36, v24, v25\n"
    "v_cvt_pk_bf16_f32 v37, v26, v27\n"
    "v_cvt_pk_bf16_f32 v38, v28, v29\n"
    "v_cvt_pk_bf16_f32 v39, v30, v31\n"
    "v_pk_mul_f32 v[58:59], v[24:25], v[100:101] op_sel_hi:[0,1]\n"
    "v_pk_mul_f32 v[60:61], v[16:17], v[96:97] op_sel_hi:[0,1]\n"
    "v_pk_mul_f32 v[62:63], v[20:21], v[96:97] op_sel_hi:[0,1]\n"
    "v_pk_mul_f32 v[12:13], v[28:29], v[96:97] op_sel_hi:[0,1]\n"
    "ds_write2_b32 v49, v36, v37 offset0:0 offset1:68\n"
    "ds_write2_b32 v49, v38, v39 offset0:136 offset1:204\n"
    "v_pk_fma_f32 v[32:33], v[24:25], v[100:101], v[58:59] op_sel:[1,1,0] op_sel_hi:[1,0,1] neg_lo:[0,1,0]\n"
    "v_pk_fma_f32 v[16:17], v[16:17], v[96:97], v[60:61] op_sel:[1,1,0] op_sel_hi:[1,0,1] neg_lo:[0,1,0]\n"
    "v_pk_fma_f32 v[20:21], v[20:21], v[96:97], v[62:63] op_sel:[1,1,0] op_sel_hi:[1,0,1] neg_lo:[0,1,0]\n"
    "v_pk_fma_f32 v[28:29], v[28:29], v[96:97], v[12:13] op_sel:[1,1,0] op_sel_hi:[1,0,1] neg_lo:[0,1,0]\n"
    "v_cvt_pk_bf16_f32 v36, v16, v17\n"
    "v_cvt_pk_bf16_f32 v37, v20, v21\n"
    "v_pk_mul_f32 v[58:59], v[24:25], v[96:97] op_sel_hi:[0,1]\n"
    "v_pk_mul_f32 v[60:61], v[32:33], v[96:97] op_sel_hi:[0,1]\n"
    "v_pk_mul_f32 v[62:63], v[16:17], v[96:97] op_sel_hi:[0,1]\n"
    "v_pk_mul_f32 v[12:13], v[96:97], v[96:97] op_sel_hi:[0,1]\n"
    "ds_write2_b32 v50, v36, v37 offset0:0 offset1:68\n"
    "v_pk_fma_f32 v[24:25], v[24:25], v[96:97], v[58:59] op_sel:[1,1,0] op_sel_hi:[1,0,1] neg_lo:[0,1,0]\n"
    "v_pk_fma_f32 v[32:33], v[32:33], v[96:97], v[60:61] op_sel:[1,1,0] op_sel_hi:[1,0,1] neg_lo:[0,1,0]\n"
    "v_pk_fma_f32 v[16:17], v[16:17], v[96:97], v[62:63] op_sel:[1,1,0] op_sel_hi:[1,0,1] neg_lo:[0,1,0]\n"
    "v_pk_fma_f32 v[98:99], v[96:97], v[96:97], v[12:13] op_sel:[1,1,0] op_sel_hi:[1,0,1] neg_lo:[0,1,0]\n"
    "v_cvt_pk_bf16_f32 v38, v24, v25\n"
    "v_cvt_pk_bf16_f32 v39, v28, v29\n"
    "v_cvt_pk_bf16_f32 v36, v32, v33\n"
    "v_cvt_pk_bf16_f32 v37, v16, v17\n"
    "v_pk_mul_f32 v[58:59], v[24:25], v[96:97] op_sel_hi:[0,1]\n"
    "v_pk_mul_f32 v[60:61], v[32:33], v[96:97] op_sel_hi:[0,1]\n"
    "v_pk_mul_f32 v[62:63], v[16:17], v[96:97] op_sel_hi:[0,1]\n"
    "ds_write2_b32 v50, v38, v39 offset0:136 offset1:204\n"
    "ds_write2_b32 v51, v36, v37 offset0:0 offset1:68\n"
    "v_pk_fma_f32 v[24:25], v[24:25], v[96:97], v[58:59] op_sel:[1,1,0] op_sel_hi:[1,0,1] neg_lo:[0,1,0]\n"
    "v_pk_fma_f32 v[32:33], v[32:33], v[96:97], v[60:61] op_sel:[1,1,0] op_sel_hi:[1,0,1] neg_lo:[0,1,0]\n"
    "v_pk_fma_f32 v[16:17], v[16:17], v[96:97], v[62:63] op_sel:[1,1,0] op_sel_hi:[1,0,1] neg_lo:[0,1,0]\n"
    "v_cvt_pk_bf16_f32 v38, v24, v25\n"
    "v_cvt_pk_bf16_f32 v39, v32, v33\n"
    "v_cvt_pk_bf16_f32 v36, v16, v17\n"
    "v_pk_mul_f32 v[58:59], v[24:25], v[96:97] op_sel_hi:[0,1]\n"
    "v_pk_mul_f32 v[60:61], v[32:33], v[96:97] op_sel_hi:[0,1]\n"
    "v_pk_mul_f32 v[62:63], v[32:33], v[98:99] op_sel_hi:[0,1]\n"
    "ds_write2_b32 v51, v38, v39 offset0:136 offset1:204\n"
    "v_pk_fma_f32 v[24:25], v[24:25], v[96:97], v[58:59] op_sel:[1,1,0] op_sel_hi:[1,0,1] neg_lo:[0,1,0]\n"
    "v_pk_fma_f32 v[30:31], v[32:33], v[96:97], v[60:61] op_sel:[1,1,0] op_sel_hi:[1,0,1] neg_lo:[0,1,0]\n"
    "v_pk_fma_f32 v[32:33], v[32:33], v[98:99], v[62:63] op_sel:[1,1,0] op_sel_hi:[1,0,1] neg_lo:[0,1,0]\n"
    "v_cvt_pk_bf16_f32 v37, v24, v25\n"
    "v_cvt_pk_bf16_f32 v38, v30, v31\n"
    "v_cvt_pk_bf16_f32 v39, v32, v33\n"
    "v_pk_mul_f32 v[58:59], v[30:31], v[98:99] op_sel_hi:[0,1]\n"
    "v_pk_mul_f32 v[60:61], v[32:33], v[98:99] op_sel_hi:[0,1]\n"
    "ds_write2_b32 v52, v36, v37 offset0:0 offset1:68\n"
    "ds_write2_b32 v52, v38, v39 offset0:136 offset1:204\n"
    "v_pk_fma_f32 v[30:31], v[30:31], v[98:99], v[58:59] op_sel:[1,1,0] op_sel_hi:[1,0,1] neg_lo:[0,1,0]\n"
    "v_pk_fma_f32 v[32:33], v[32:33], v[98:99], v[60:61] op_sel:[1,1,0] op_sel_hi:[1,0,1] neg_lo:[0,1,0]\n"
    "v_cvt_pk_bf16_f32 v36, v30, v31\n"
    "v_cvt_pk_bf16_f32 v37, v32, v33\n"
    "v_pk_mul_f32 v[58:59], v[30:31], v[98:99] op_sel_hi:[0,1]\n"
    "ds_write2_b32 v53, v36, v37 offset0:0 offset1:68\n"
    "v_pk_fma_f32 v[30:31], v[30:31], v[98:99], v[58:59] op_sel:[1,1,0] op_sel_hi:[1,0,1] neg_lo:[0,1,0]\n"
    "s_nop 0\n"
    "v_cvt_pk_bf16_f32 v38, v30, v31\n"
    "ds_write_b32 v53, v38 offset:544\n"
    "ds_read_b128 v[96:99], v56\n"
    "ds_read_b128 v[100:103], v56 offset:32\n"
    "ds_read_b128 v[104:107], v56 offset:64\n"
    "ds_read_b128 v[108:111], v56 offset:96\n"
    "ds_read_b128 v[112:115], v56 offset:128\n"
    "ds_read_b128 v[116:119], v56 offset:160\n"
    "ds_read_b128 v[120:123], v56 offset:192\n"
    "ds_read_b128 v[124:127], v56 offset:224\n"
    "s_mul_i32 s50, s41, 3840\n"
    "s_add_u32 s50, s50, 156000\n"
    "v_lshl_add_u32 v40, v1, 4, s50\n"
    "v_lshl_add_u32 v42, v1, 2, s50\n"
    "s_waitcnt lgkmcnt(7)\n"
    "v_mfma_f32_32x32x16_bf16 v[8:23], v[96:99], v[64:67], 0\n"
    "s_waitcnt lgkmcnt(6)\n"
    "v_mfma_f32_32x32x16_bf16 v[8:23], v[100:103], v[68:71], v[8:23]\n"
    "s_waitcnt lgkmcnt(5)\n"
    "v_mfma_f32_32x32x16_bf16 v[8:23], v[104:107], v[72:75], v[8:23]\n"
    "s_waitcnt lgkmcnt(4)\n"
    "v_mfma_f32_32x32x16_bf16 v[8:23], v[108:111], v[76:79], v[8:23]\n"
    "s_waitcnt lgkmcnt(3)\n"
    "v_mfma_f32_32x32x16_bf16 v[8:23], v[112:115], v[80:83], v[8:23]\n"
    "s_waitcnt lgkmcnt(2)\n"
    "v_mfma_f32_32x32x16_bf16 v[8:23], v[116:119], v[84:87], v[8:23]\n"
    "s_waitcnt lgkmcnt(1)\n"
    "v_mfma_f32_32x32x16_bf16 v[8:23], v[120:123], v[88:91], v[8:23]\n"
    "s_waitcnt lgkmcnt(0)\n"
    "v_mfma_f32_32x32x16_bf16 v[8:23], v[124:127], v[92:95], v[8:23]\n"
    "s_cmp_ge_u32 s40, 4\n"
    "s_cbranch_scc1 .Lk_h67\n"
    "s_nop 9\n"
    "ds_write_b128 v40, v[8:11] offset:0\n"
    "ds_write_b128 v40, v[12:15] offset:1024\n"
    "ds_write_b128 v40, v[16:19] offset:2048\n"
    "ds_write_b32 v42, v20 offset:3072\n"
    ".Lk_gmain:\n"
    "s_waitcnt vmcnt(0) lgkmcnt(0)\n"
    "s_cmp_eq_u32 s45, 15\n"
    "s_cbranch_scc1 .Lk_gates\n"
    "s_mov_b32 s50, 0\n"
    "s_mov_b32 s51, 7\n"
    "v_mov_b32 v82, 163776\n"
    "s_barrier\n"
    "ds_read_b64 v[80:81], v82\n"
    "ds_read_b128 v[8:11], v224\n"
    "ds_read_b64_tr_b16 v[24:25], v228\n"
    "ds_read_b64_tr_b16 v[26:27], v228 offset:768\n"
    "ds_read_b128 v[12:15], v224 offset:4096\n"
    "ds_read_b64_tr_b16 v[28:29], v228 offset:32\n"
    "ds_read_b64_tr_b16 v[30:31], v228 offset:800\n"
    "ds_read_b64_tr_b16 v[32:33], v228 offset:64\n"
    "ds_read_b64_tr_b16 v[34:35], v228 offset:832\n"
    ".p2align 3 \n"
    ".Lk_g1:\n"
    "s_waitcnt lgkmcnt(5)\n"
    "v_mfma_f32_32x32x16_bf16 v[128:143], v[24:27], v[8:11], v[128:143]\n"
    "ds_read_b128 v[16:19], v225\n"
    "ds_read_b64_tr_b16 v[36:37], v228 offset:3072\n"
    "ds_read_b64_tr_b16 v[38:39], v228 offset:3840\n"
    "s_waitcnt lgkmcnt(7)\n"
    "v_mfma_f32_32x32x16_bf16 v[144:159], v[24:27], v[12:15], v[144:159]\n"
    "ds_read_b128 v[20:23], v225 offset:4096\n"
    "s_waitcnt lgkmcnt(6)\n"
    "v_mfma_f32_32x32x16_bf16 v[160:175], v[28:31], v[8:11], v[160:175]\n"
    "ds_read_b64_tr_b16 v[40:41], v228 offset:3104\n"
    "ds_read_b64_tr_b16 v[42:43], v228 offset:3872\n"
    "v_mfma_f32_32x32x16_bf16 v[176:191], v[28:31], v[12:15], v[176:191]\n"
    "ds_read_b64_tr_b16 v[44:45], v228 offset:3136\n"
    "ds_read_b64_tr_b16 v[46:47], v228 offset:3904\n"
    "s_waitcnt lgkmcnt(8)\n"
    "v_mfma_f32_32x32x16_bf16 v[192:207], v[32:35], v[8:11], v[192:207]\n"
    "v_mfma_f32_32x32x16_bf16 v[208:223], v[32:35], v[12:15], v[208:223]\n"
    "s_waitcnt lgkmcnt(5)\n"
    "v_mfma_f32_32x32x16_bf16 v[128:143], v[36:39], v[16:19], v[128:143]\n"
    "ds_read_b128 v[8:11], v226\n"
    "ds_read_b64_tr_b16 v[24:25], v228 offset:6144\n"
    "ds_read_b64_tr_b16 v[26:27], v228 offset:6912\n"
    "s_waitcnt lgkmcnt(7)\n"
    "v_mfma_f32_32x32x16_bf16 v[144:159], v[36:39], v[20:23], v[144:159]\n"
    "ds_read_b128 v[12:15], v226 offset:4096\n"
    "s_waitcnt lgkmcnt(6)\n"
    "v_mfma_f32_32x32x16_bf16 v[160:175], v[40:43], v[16:19], v[160:175]\n"
    "ds_read_b64_tr_b16 v[28:29], v228 offset:6176\n"
    "ds_read_b64_tr_b16 v[30:31], v228 offset:6944\n"
    "v_mfma_f32_32x32x16_bf16 v[176:191], v[40:43], v[20:23], v[176:191]\n"
    "ds_read_b64_tr_b16 v[32:33], v228 offset:6208\n"
    "ds_read_b64_tr_b16 v[34:35], v228 offset:6976\n"
    "s_waitcnt lgkmcnt(8)\n"
    "v_mfma_f32_32x32x16_bf16 v[192:207], v[44:47], v[16:19], v[192:207]\n"
    "s_cmp_eq_u32 s50, 81984\n"
    "s_cselect_b32 s52, 122976, 0\n"
    "s_sub_u32 s52, 40992, s52\n"
    "s_add_u32 s50, s50, s52\n"
    "v_mfma_f32_32x32x16_bf16 v[208:223], v[44:47], v[20:23], v[208:223]\n"
    "s_waitcnt lgkmcnt(5)\n"
    "v_mfma_f32_32x32x16_bf16 v[128:143], v[24:27], v[8:11], v[128:143]\n"
    "ds_read_b128 v[16:19], v227\n"
    "ds_read_b64_tr_b16 v[36:37], v228 offset:9216\n"
    "ds_read_b64_tr_b16 v[38:39], v228 offset:9984\n"
    "s_waitcnt lgkmcnt(7)\n"
    "v_mfma_f32_32x32x16_bf16 v[144:159], v[24:27], v[12:15], v[144:159]\n"
    "ds_read_b128 v[20:23], v227 offset:4096\n"
    "ds_read_b64_tr_b16 v[40:41], v228 offset:9248\n"
    "ds_read_b64_tr_b16 v[42:43], v228 offset:10016\n"
    "s_waitcnt lgkmcnt(8)\n"
    "v_mfma_f32_32x32x16_bf16 v[160:175], v[28:31], v[8:11], v[160:175]\n"
    "ds_read_b64_tr_b16 v[44:45], v228 offset:9280\n"
    "ds_read_b64_tr_b16 v[46:47], v228 offset:10048\n"
    "v_mfma_f32_32x32x16_bf16 v[176:191], v[28:31], v[12:15], v[176:191]\n"
    "v_add_u32 v224, s52, v224\n"
    "v_add_u32 v225, s52, v225\n"
    "v_add_u32 v226, s52, v226\n"
    "s_waitcnt lgkmcnt(8)\n"
    "v_mfma_f32_32x32x16_bf16 v[192:207], v[32:35], v[8:11], v[192:207]\n"
    "v_add_u32 v227, s52, v227\n"
    "v_add_u32 v228, s52, v228\n"
    "s_sub_u32 s51, s51, 1\n"
    "v_mfma_f32_32x32x16_bf16 v[208:223], v[32:35], v[12:15], v[208:223]\n"
    "s_waitcnt lgkmcnt(0)\n"
    "v_mfma_f32_32x32x16_bf16 v[128:143], v[36:39], v[16:19], v[128:143]\n"
    "s_cbranch_scc1 .Lk_g1x\n"
    "s_barrier\n"
    "v_mfma_f32_32x32x16_bf16 v[144:159], v[36:39], v[20:23], v[144:159]\n"
    "ds_read_b128 v[8:11], v224\n"
    "ds_read_b64_tr_b16 v[24:25], v228\n"
    "ds_read_b64_tr_b16 v[26:27], v228 offset:768\n"
    "v_mfma_f32_32x32x16_bf16 v[160:175], v[40:43], v[16:19], v[160:175]\n"
    "ds_read_b128 v[12:15], v224 offset:4096\n"
    "v_mfma_f32_32x32x16_bf16 v[176:191], v[40:43], v[20:23], v[176:191]\n"
    "ds_read_b64_tr_b16 v[28:29], v228 offset:32\n"
    "ds_read_b64_tr_b16 v[30:31], v228 offset:800\n"
    "v_mfma_f32_32x32x16_bf16 v[192:207], v[44:47], v[16:19], v[192:207]\n"
    "ds_read_b64_tr_b16 v[32:33], v228 offset:64\n"
    "ds_read_b64_tr_b16 v[34:35], v228 offset:832\n"
    "v_mfma_f32_32x32x16_bf16 v[208:223], v[44:47], v[20:23], v[208:223]\n"
    "s_branch .Lk_g1\n"
    ".Lk_g1x:\n"
    "v_mfma_f32_32x32x16_bf16 v[144:159], v[36:39], v[20:23], v[144:159]\n"
    "v_mfma_f32_32x32x16_bf16 v[160:175], v[40:43], v[16:19], v[160:175]\n"
    "s_mov_b32 s50, 0xbdd2d3e8\n"
    "v_mov_b32 v6, 8\n"
    "v_mfma_f32_32x32x16_bf16 v[176:191], v[40:43], v[20:23], v[176:191]\n"
    "v_mfma_f32_32x32x16_bf16 v[192:207], v[44:47], v[16:19], v[192:207]\n"
    "v_mfma_f32_32x32x16_bf16 v[208:223], v[44:47], v[20:23], v[208:223]\n"
    "ds_write_b32 v239, v6\n"
    "s_nop 2\n"
    ".p2align 3 \n"
    "v_pk_mul_f32 v[56:57], v[128:129], v[128:129]\n"
    "v_pk_mul_f32 v[58:59], v[130:131], v[130:131]\n"
    "v_pk_mul_f32 v[60:61], v[132:133], v[132:133]\n"
    "v_pk_mul_f32 v[62:63], v[134:135], v[134:135]\n"
    "v_pk_fma_f32 v[56:57], v[56:57], s[50:51], v[242:243] op_sel_hi:[1,0,0]\n"
    "v_pk_fma_f32 v[58:59], v[58:59], s[50:51], v[242:243] op_sel_hi:[1,0,0]\n"
    "v_pk_fma_f32 v[60:61], v[60:61], s[50:51], v[242:243] op_sel_hi:[1,0,0]\n"
    "v_pk_fma_f32 v[62:63], v[62:63], s[50:51], v[242:243] op_sel_hi:[1,0,0]\n"
    "v_pk_mul_f32 v[56:57], v[56:57], v[128:129]\n"
    "v_pk_mul_f32 v[58:59], v[58:59], v[130:131]\n"
    "v_pk_mul_f32 v[60:61], v[60:61], v[132:133]\n"
    "v_pk_mul_f32 v[62:63], v[62:63], v[134:135]\n"
    "v_pk_mul_f32 v[128:129], v[128:129], v[136:137]\n"
    "v_pk_mul_f32 v[130:131], v[130:131], v[138:139]\n"
    "v_pk_mul_f32 v[132:133], v[132:133], v[140:141]\n"
    "v_pk_mul_f32 v[134:135], v[134:135], v[142:143]\n"
    "v_exp_f32 v56, v56\n"
    "v_exp_f32 v57, v57\n"
    "v_exp_f32 v58, v58\n"
    "v_exp_f32 v59, v59\n"
    "v_exp_f32 v60, v60\n"
    "v_exp_f32 v61, v61\n"
    "v_exp_f32 v62, v62\n"
    "v_exp_f32 v63, v63\n"
    "v_pk_add_f32 v[56:57], v[56:57], 1.0 op_sel_hi:[1,0]\n"
    "v_pk_add_f32 v[58:59], v[58:59], 1.0 op_sel_hi:[1,0]\n"
    "v_pk_add_f32 v[60:61], v[60:61], 1.0 op_sel_hi:[1,0]\n"
    "v_pk_add_f32 v[62:63], v[62:63], 1.0 op_sel_hi:[1,0]\n"
    "v_rcp_f32 v56, v56\n"
    "v_rcp_f32 v57, v57\n"
    "v_rcp_f32 v58, v58\n"
    "v_rcp_f32 v59, v59\n"
    "v_rcp_f32 v60, v60\n"
    "v_rcp_f32 v61, v61\n"
    "v_rcp_f32 v62, v62\n"
    "v_rcp_f32 v63, v63\n"
    "v_pk_mul_f32 v[56:57], v[56:57], v[128:129]\n"
    "v_pk_mul_f32 v[58:59], v[58:59], v[130:131]\n"
    "v_pk_mul_f32 v[60:61], v[60:61], v[132:133]\n"
    "v_pk_mul_f32 v[62:63], v[62:63], v[134:135]\n"
    "v_cvt_pk_bf16_f32 v140, v56, v57\n"
    "v_cvt_pk_bf16_f32 v141, v58, v59\n"
    "v_cvt_pk_bf16_f32 v142, v60, v61\n"
    "v_cvt_pk_bf16_f32 v143, v62, v63\n"
    "buffer_store_dwordx4 v[140:143], v240, s[12:15], s60 offen sc1\n"
    "v_pk_mul_f32 v[56:57], v[144:145], v[144:145]\n"
    "v_pk_mul_f32 v[58:59], v[146:147], v[146:147]\n"
    "v_pk_mul_f32 v[60:61], v[148:149], v[148:149]\n"
    "v_pk_mul_f32 v[62:63], v[150:151], v[150:151]\n"
    "v_pk_fma_f32 v[56:57], v[56:57], s[50:51], v[242:243] op_sel_hi:[1,0,0]\n"
    "v_pk_fma_f32 v[58:59], v[58:59], s[50:51], v[242:243] op_sel_hi:[1,0,0]\n"
    "v_pk_fma_f32 v[60:61], v[60:61], s[50:51], v[242:243] op_sel_hi:[1,0,0]\n"
    "v_pk_fma_f32 v[62:63], v[62:63], s[50:51], v[242:243] op_sel_hi:[1,0,0]\n"
    "v_pk_mul_f32 v[56:57], v[56:57], v[144:145]\n"
    "v_pk_mul_f32 v[58:59], v[58:59], v[146:147]\n"
    "v_pk_mul_f32 v[60:61], v[60:61], v[148:149]\n"
    "v_pk_mul_f32 v[62:63], v[62:63], v[150:151]\n"
    "v_pk_mul_f32 v[144:145], v[144:145], v[152:153]\n"
    "v_pk_mul_f32 v[146:147], v[146:147], v[154:155]\n"
    "v_pk_mul_f32 v[148:149], v[148:149], v[156:157]\n"
    "v_pk_mul_f32 v[150:151], v[150:151], v[158:159]\n"
    "v_exp_f32 v56, v56\n"
    "v_exp_f32 v57, v57\n"
    "v_exp_f32 v58, v58\n"
    "v_exp_f32 v59, v59\n"
    "v_exp_f32 v60, v60\n"
    "v_exp_f32 v61, v61\n"
    "v_exp_f32 v62, v62\n"
    "v_exp_f32 v63, v63\n"
    "v_pk_add_f32 v[56:57], v[56:57], 1.0 op_sel_hi:[1,0]\n"
    "v_pk_add_f32 v[58:59], v[58:59], 1.0 op_sel_hi:[1,0]\n"
    "v_pk_add_f32 v[60:61], v[60:61], 1.0 op_sel_hi:[1,0]\n"
    "v_pk_add_f32 v[62:63], v[62:63], 1.0 op_sel_hi:[1,0]\n"
    "v_rcp_f32 v56, v56\n"
    "v_rcp_f32 v57, v57\n"
    "v_rcp_f32 v58, v58\n"
    "v_rcp_f32 v59, v59\n"
    "v_rcp_f32 v60, v60\n"
    "v_rcp_f32 v61, v61\n"
    "v_rcp_f32 v62, v62\n"
    "v_rcp_f32 v63, v63\n"
    "v_pk_mul_f32 v[56:57], v[56:57], v[144:145]\n"
    "v_pk_mul_f32 v[58:59], v[58:59], v[146:147]\n"
    "v_pk_mul_f32 v[60:61], v[60:61], v[148:149]\n"
    "v_pk_mul_f32 v[62:63], v[62:63], v[150:151]\n"
    "v_cvt_pk_bf16_f32 v156, v56, v57\n"
    "v_cvt_pk_bf16_f32 v157, v58, v59\n"
    "v_cvt_pk_bf16_f32 v158, v60, v61\n"
    "v_cvt_pk_bf16_f32 v159, v62, v63\n"
    "buffer_store_dwordx4 v[156:159], v241, s[12:15], s60 offen sc1\n"
    "v_pk_mul_f32 v[56:57], v[160:161], v[160:161]\n"
    "v_pk_mul_f32 v[58:59], v[162:163], v[162:163]\n"
    "v_pk_mul_f32 v[60:61], v[164:165], v[164:165]\n"
    "v_pk_mul_f32 v[62:63], v[166:167], v[166:167]\n"
    "v_pk_fma_f32 v[56:57], v[56:57], s[50:51], v[242:243] op_sel_hi:[1,0,0]\n"
    "v_pk_fma_f32 v[58:59], v[58:59], s[50:51], v[242:243] op_sel_hi:[1,0,0]\n"
    "v_pk_fma_f32 v[60:61], v[60:61], s[50:51], v[242:243] op_sel_hi:[1,0,0]\n"
    "v_pk_fma_f32 v[62:63], v[62:63], s[50:51], v[242:243] op_sel_hi:[1,0,0]\n"
    "v_pk_mul_f32 v[56:57], v[56:57], v[160:161]\n"
    "v_pk_mul_f32 v[58:59], v[58:59], v[162:163]\n"
    "v_pk_mul_f32 v[60:61], v[60:61], v[164:165]\n"
    "v_pk_mul_f32 v[62:63], v[62:63], v[166:167]\n"
    "v_pk_mul_f32 v[160:161], v[160:161], v[168:169]\n"
    "v_pk_mul_f32 v[162:163], v[162:163], v[170:171]\n"
    "v_pk_mul_f32 v[164:165], v[164:165], v[172:173]\n"
    "v_pk_mul_f32 v[166:167], v[166:167], v[174:175]\n"
    "v_exp_f32 v56, v56\n"
    "v_exp_f32 v57, v57\n"
    "v_exp_f32 v58, v58\n"
    "v_exp_f32 v59, v59\n"
    "v_exp_f32 v60, v60\n"
    "v_exp_f32 v61, v61\n"
    "v_exp_f32 v62, v62\n"
    "v_exp_f32 v63, v63\n"
    "v_pk_add_f32 v[56:57], v[56:57], 1.0 op_sel_hi:[1,0]\n"
    "v_pk_add_f32 v[58:59], v[58:59], 1.0 op_sel_hi:[1,0]\n"
    "v_pk_add_f32 v[60:61], v[60:61], 1.0 op_sel_hi:[1,0]\n"
    "v_pk_add_f32 v[62:63], v[62:63], 1.0 op_sel_hi:[1,0]\n"
    "v_rcp_f32 v56, v56\n"
    "v_rcp_f32 v57, v57\n"
    "v_rcp_f32 v58, v58\n"
    "v_rcp_f32 v59, v59\n"
    "v_rcp_f32 v60, v60\n"
    "v_rcp_f32 v61, v61\n"
    "v_rcp_f32 v62, v62\n"
    "v_rcp_f32 v63, v63\n"
    "v_pk_mul_f32 v[56:57], v[56:57], v[160:161]\n"
    "v_pk_mul_f32 v[58:59], v[58:59], v[162:163]\n"
    "v_pk_mul_f32 v[60:61], v[60:61], v[164:165]\n"
    "v_pk_mul_f32 v[62:63], v[62:63], v[166:167]\n"
    "v_cvt_pk_bf16_f32 v172, v56, v57\n"
    "v_cvt_pk_bf16_f32 v173, v58, v59\n"
    "v_cvt_pk_bf16_f32 v174, v60, v61\n"
    "v_cvt_pk_bf16_f32 v175, v62, v63\n"
    "buffer_store_dwordx4 v[172:175], v240, s[12:15], s61 offen sc1\n"
    "v_pk_mul_f32 v[56:57], v[176:177], v[176:177]\n"
    "v_pk_mul_f32 v[58:59], v[178:179], v[178:179]\n"
    "v_pk_mul_f32 v[60:61], v[180:181], v[180:181]\n"
    "v_pk_mul_f32 v[62:63], v[182:183], v[182:183]\n"
    "v_pk_fma_f32 v[56:57], v[56:57], s[50:51], v[242:243] op_sel_hi:[1,0,0]\n"
    "v_pk_fma_f32 v[58:59], v[58:59], s[50:51], v[242:243] op_sel_hi:[1,0,0]\n"
    "v_pk_fma_f32 v[60:61], v[60:61], s[50:51], v[242:243] op_sel_hi:[1,0,0]\n"
    "v_pk_fma_f32 v[62:63], v[62:63], s[50:51], v[242:243] op_sel_hi:[1,0,0]\n"
    "v_pk_mul_f32 v[56:57], v[56:57], v[176:177]\n"
    "v_pk_mul_f32 v[58:59], v[58:59], v[178:179]\n"
    "v_pk_mul_f32 v[60:61], v[60:61], v[180:181]\n"
    "v_pk_mul_f32 v[62:63], v[62:63], v[182:183]\n"
    "v_pk_mul_f32 v[176:177], v[176:177], v[184:185]\n"
    "v_pk_mul_f32 v[178:179], v[178:179], v[186:187]\n"
    "v_pk_mul_f32 v[180:181], v[180:181], v[188:189]\n"
    "v_pk_mul_f32 v[182:183], v[182:183], v[190:191]\n"
    "v_exp_f32 v56, v56\n"
    "v_exp_f32 v57, v57\n"
    "v_exp_f32 v58, v58\n"
    "v_exp_f32 v59, v59\n"
    "v_exp_f32 v60, v60\n"
    "v_exp_f32 v61, v61\n"
    "v_exp_f32 v62, v62\n"
    "v_exp_f32 v63, v63\n"
    "v_pk_add_f32 v[56:57], v[56:57], 1.0 op_sel_hi:[1,0]\n"
    "v_pk_add_f32 v[58:59], v[58:59], 1.0 op_sel_hi:[1,0]\n"
    "v_pk_add_f32 v[60:61], v[60:61], 1.0 op_sel_hi:[1,0]\n"
    "v_pk_add_f32 v[62:63], v[62:63], 1.0 op_sel_hi:[1,0]\n"
    "v_rcp_f32 v56, v56\n"
    "v_rcp_f32 v57, v57\n"
    "v_rcp_f32 v58, v58\n"
    "v_rcp_f32 v59, v59\n"
    "v_rcp_f32 v60, v60\n"
    "v_rcp_f32 v61, v61\n"
    "v_rcp_f32 v62, v62\n"
    "v_rcp_f32 v63, v63\n"
    "v_pk_mul_f32 v[56:57], v[56:57], v[176:177]\n"
    "v_pk_mul_f32 v[58:59], v[58:59], v[178:179]\n"
    "v_pk_mul_f32 v[60:61], v[60:61], v[180:181]\n"
    "v_pk_mul_f32 v[62:63], v[62:63], v[182:183]\n"
    "v_cvt_pk_bf16_f32 v188, v56, v57\n"
    "v_cvt_pk_bf16_f32 v189, v58, v59\n"
    "v_cvt_pk_bf16_f32 v190, v60, v61\n"
    "v_cvt_pk_bf16_f32 v191, v62, v63\n"
    "buffer_store_dwordx4 v[188:191], v241, s[12:15], s61 offen sc1\n"
    "ds_read_b32 v85, v238\n"
    "v_pk_mul_f32 v[56:57], v[192:193], v[192:193]\n"
    "v_pk_mul_f32 v[58:59], v[194:195], v[194:195]\n"
    "v_pk_mul_f32 v[60:61], v[196:197], v[196:197]\n"
    "v_pk_mul_f32 v[62:63], v[198:199], v[198:199]\n"
    "v_pk_fma_f32 v[56:57], v[56:57], s[50:51], v[242:243] op_sel_hi:[1,0,0]\n"
    "v_pk_fma_f32 v[58:59], v[58:59], s[50:51], v[242:243] op_sel_hi:[1,0,0]\n"
    "v_pk_fma_f32 v[60:61], v[60:61], s[50:51], v[242:243] op_sel_hi:[1,0,0]\n"
    "v_pk_fma_f32 v[62:63], v[62:63], s[50:51], v[242:243] op_sel_hi:[1,0,0]\n"
    "v_pk_mul_f32 v[56:57], v[56:57], v[192:193]\n"
    "v_pk_mul_f32 v[58:59], v[58:59], v[194:195]\n"
    "v_pk_mul_f32 v[60:61], v[60:61], v[196:197]\n"
    "v_pk_mul_f32 v[62:63], v[62:63], v[198:199]\n"
    "v_pk_mul_f32 v[192:193], v[192:193], v[200:201]\n"
    "v_pk_mul_f32 v[194:195], v[194:195], v[202:203]\n"
    "v_pk_mul_f32 v[196:197], v[196:197], v[204:205]\n"
    "v_pk_mul_f32 v[198:199], v[198:199], v[206:207]\n"
    "v_exp_f32 v56, v56\n"
    "v_exp_f32 v57, v57\n"
    "v_exp_f32 v58, v58\n"
    "v_exp_f32 v59, v59\n"
    "v_exp_f32 v60, v60\n"
    "v_exp_f32 v61, v61\n"
    "v_exp_f32 v62, v62\n"
    "v_exp_f32 v63, v63\n"
    "v_pk_add_f32 v[56:57], v[56:57], 1.0 op_sel_hi:[1,0]\n"
    "v_pk_add_f32 v[58:59], v[58:59], 1.0 op_sel_hi:[1,0]\n"
    "v_pk_add_f32 v[60:61], v[60:61], 1.0 op_sel_hi:[1,0]\n"
    "v_pk_add_f32 v[62:63], v[62:63], 1.0 op_sel_hi:[1,0]\n"
    "v_rcp_f32 v56, v56\n"
    "v_rcp_f32 v57, v57\n"
    "v_rcp_f32 v58, v58\n"
    "v_rcp_f32 v59, v59\n"
    "v_rcp_f32 v60, v60\n"
    "v_rcp_f32 v61, v61\n"
    "v_rcp_f32 v62, v62\n"
    "v_rcp_f32 v63, v63\n"
    "v_pk_mul_f32 v[56:57], v[56:57], v[192:193]\n"
    "v_pk_mul_f32 v[58:59], v[58:59], v[194:195]\n"
    "v_pk_mul_f32 v[60:61], v[60:61], v[196:197]\n"
    "v_pk_mul_f32 v[62:63], v[62:63], v[198:199]\n"
    "v_cvt_pk_bf16_f32 v204, v56, v57\n"
    "v_cvt_pk_bf16_f32 v205, v58, v59\n"
    "v_cvt_pk_bf16_f32 v206, v60, v61\n"
    "v_cvt_pk_bf16_f32 v207, v62, v63\n"
    "buffer_store_dwordx4 v[204:207], v240, s[12:15], s62 offen sc1\n"
    "v_pk_mul_f32 v[56:57], v[208:209], v[208:209]\n"
    "v_pk_mul_f32 v[58:59], v[210:211], v[210:211]\n"
    "v_pk_mul_f32 v[60:61], v[212:213], v[212:213]\n"
    "v_pk_mul_f32 v[62:63], v[214:215], v[214:215]\n"
    "v_pk_fma_f32 v[56:57], v[56:57], s[50:51], v[242:243] op_sel_hi:[1,0,0]\n"
    "v_pk_fma_f32 v[58:59], v[58:59], s[50:51], v[242:243] op_sel_hi:[1,0,0]\n"
    "v_pk_fma_f32 v[60:61], v[60:61], s[50:51], v[242:243] op_sel_hi:[1,0,0]\n"
    "v_pk_fma_f32 v[62:63], v[62:63], s[50:51], v[242:243] op_sel_hi:[1,0,0]\n"
    "v_pk_mul_f32 v[56:57], v[56:57], v[208:209]\n"
    "v_pk_mul_f32 v[58:59], v[58:59], v[210:211]\n"
    "v_pk_mul_f32 v[60:61], v[60:61], v[212:213]\n"
    "v_pk_mul_f32 v[62:63], v[62:63], v[214:215]\n"
    "v_pk_mul_f32 v[208:209], v[208:209], v[216:217]\n"
    "v_pk_mul_f32 v[210:211], v[210:211], v[218:219]\n"
    "v_pk_mul_f32 v[212:213], v[212:213], v[220:221]\n"
    "v_pk_mul_f32 v[214:215], v[214:215], v[222:223]\n"
    "v_exp_f32 v56, v56\n"
    "v_exp_f32 v57, v57\n"
    "v_exp_f32 v58, v58\n"
    "v_exp_f32 v59, v59\n"
    "v_exp_f32 v60, v60\n"
    "v_exp_f32 v61, v61\n"
    "v_exp_f32 v62, v62\n"
    "v_exp_f32 v63, v63\n"
    "v_pk_add_f32 v[56:57], v[56:57], 1.0 op_sel_hi:[1,0]\n"
    "v_pk_add_f32 v[58:59], v[58:59], 1.0 op_sel_hi:[1,0]\n"
    "v_pk_add_f32 v[60:61], v[60:61], 1.0 op_sel_hi:[1,0]\n"
    "v_pk_add_f32 v[62:63], v[62:63], 1.0 op_sel_hi:[1,0]\n"
    "v_rcp_f32 v56, v56\n"
    "v_rcp_f32 v57, v57\n"
    "v_rcp_f32 v58, v58\n"
    "v_rcp_f32 v59, v59\n"
    "v_rcp_f32 v60, v60\n"
    "v_rcp_f32 v61, v61\n"
    "v_rcp_f32 v62, v62\n"
    "v_rcp_f32 v63, v63\n"
    "v_pk_mul_f32 v[56:57], v[56:57], v[208:209]\n"
    "v_pk_mul_f32 v[58:59], v[58:59], v[210:211]\n"
    "v_pk_mul_f32 v[60:61], v[60:61], v[212:213]\n"
    "v_pk_mul_f32 v[62:63], v[62:63], v[214:215]\n"
    "v_cvt_pk_bf16_f32 v220, v56, v57\n"
    "v_cvt_pk_bf16_f32 v221, v58, v59\n"
    "v_cvt_pk_bf16_f32 v222, v60, v61\n"
    "v_cvt_pk_bf16_f32 v223, v62, v63\n"
    "buffer_store_dwordx4 v[220:223], v241, s[12:15], s62 offen sc1\n"
    "s_waitcnt lgkmcnt(0)\n"
    "v_cmp_gt_u32 vcc, v244, v85\n"
    "s_and_b64 vcc, exec, vcc\n"
    "s_cbranch_vccz .Lk_gpre\n"
    ".Lk_gtail:\n"
    "s_mov_b32 s69, 4194304\n"
    ".Lk_l4:\n"
    "ds_read_b32 v85, v238\n"
    "s_waitcnt lgkmcnt(0)\n"
    "v_cmp_gt_u32 vcc, v244, v85\n"
    "s_and_b64 vcc, exec, vcc\n"
    "s_cbranch_vccz .Lk_l5\n"
    "s_sleep 1\n"
    "s_sub_u32 s69, s69, 1\n"
    "s_cbranch_scc0 .Lk_l4\n"
    ".Lk_l5:\n"
    ".Lk_gpre:\n"
    "v_mov_b32 v6, 9\n"
    "ds_write_b32 v239, v6\n"
    "ds_read_b64_tr_b16 v[8:9], v232\n"
    "ds_read_b64_tr_b16 v[10:11], v232 offset:512\n"
    "ds_read_b64_tr_b16 v[12:13], v233\n"
    "ds_read_b64_tr_b16 v[14:15], v233 offset:512\n"
    "ds_read_b64_tr_b16 v[16:17], v232 offset:4096\n"
    "ds_read_b64_tr_b16 v[18:19], v232 offset:4608\n"
    "ds_read_b64_tr_b16 v[20:21], v233 offset:4096\n"
    "ds_read_b64_tr_b16 v[22:23], v233 offset:4608\n"
    "ds_read_b64_tr_b16 v[24:25], v232 offset:8192\n"
    "ds_read_b64_tr_b16 v[26:27], v232 offset:8704\n"
    "ds_read_b64_tr_b16 v[28:29], v233 offset:8192\n"
    "ds_read_b64_tr_b16 v[30:31], v233 offset:8704\n"
    "ds_read_b64_tr_b16 v[32:33], v232 offset:12288\n"
    "ds_read_b64_tr_b16 v[34:35], v232 offset:12800\n"
    "ds_read_b64_tr_b16 v[36:37], v233 offset:12288\n"
    "ds_read_b64_tr_b16 v[38:39], v233 offset:12800\n"
    "ds_read_b64_tr_b16 v[40:41], v232 offset:16384\n"
    "ds_read_b64_tr_b16 v[42:43], v232 offset:16896\n"
    "ds_read_b64_tr_b16 v[44:45], v233 offset:16384\n"
    "ds_read_b64_tr_b16 v[46:47], v233 offset:16896\n"
    "ds_read_b64_tr_b16 v[48:49], v232 offset:20480\n"
    "ds_read_b64_tr_b16 v[50:51], v232 offset:20992\n"
    "ds_read_b64_tr_b16 v[52:53], v233 offset:20480\n"
    "ds_read_b64_tr_b16 v[54:55], v233 offset:20992\n"
    "ds_read_b64_tr_b16 v[64:65], v232 offset:24576\n"
    "ds_read_b64_tr_b16 v[66:67], v232 offset:25088\n"
    "ds_read_b64_tr_b16 v[68:69], v233 offset:24576\n"
    "ds_read_b64_tr_b16 v[70:71], v233 offset:25088\n"
    "ds_read_b64_tr_b16 v[76:77], v232 offset:28672\n"
    "ds_read_b64_tr_b16 v[78:79], v232 offset:29184\n"
    "ds_read_b64_tr_b16 v[96:97], v233 offset:28672\n"
    "ds_read_b64_tr_b16 v[98:99], v233 offset:29184\n"
    "v_mov_b32 v6, 10\n"
    "ds_write_b32 v239, v6\n"
    "s_mov_b32 s62, 1\n"
    ".Lk_l6:\n"
    "s_mov_b32 m0, s29\n"
    "s_nop 0\n"
    "buffer_load_dwordx4 v234, s[24:27], s28 offen lds\n"
    "buffer_load_dwordx4 v235, s[24:27], s28 offen offset:1024 lds\n"
    "buffer_load_dwordx4 v236, s[24:27], s28 offen offset:2048 lds\n"
    "buffer_load_dwordx4 v237, s[24:27], s28 offen offset:3072 lds\n"
    "s_add_u32 s28, s28, 131072\n"
    "s_add_u32 s29, s29, 16384\n"
    "s_sub_u32 s62, s62, 1\n"
    "s_cbranch_scc0 .Lk_l6\n"
    "v_mov_b64 v[72:73], v[230:231]\n"
    "v_mov_b32 v2, v239\n"
    "v_mov_b32 v4, v240\n"
    "v_and_b32 v3, 3, v1\n"
    "v_lshlrev_b32 v3, 2, v3\n"
    "v_add_u32 v3, 163808, v3\n"
    "s_lshr_b32 s52, s40, 1\n"
    "s_cmp_eq_u32 s40, 3\n"
    "s_cselect_b32 s62, 2, 3\n"
    "s_sub_u32 s29, s29, 4096\n"
    "s_sub_u32 s28, s28, 32768\n"
    "s_waitcnt vmcnt(8)\n"
    "s_mov_b64 exec, 1\n"
    "buffer_atomic_swap v80, off, s[12:15], s65\n"
    "s_mov_b64 exec, -1\n"
    "v_readfirstlane_b32 s66, v80\n"
    "v_readfirstlane_b32 s67, v81\n"
    "s_lshl1_add_u32 s50, s46, s41\n"
    "s_mul_i32 s60, s50, 86\n"
    "s_add_u32 s60, s60, s52\n"
    "s_bitcmp1_b32 s47, 0\n"
    "s_cselect_b32 s51, 0, 0\n"
    "s_cselect_b32 s63, 0, 0\n"
    "s_add_u32 s60, s60, s51\n"
    "s_add_u32 s63, s63, 2048\n"
    "s_lshl_b32 s60, s60, 10\n"
    "s_add_u32 s60, s60, 2621440\n"
    "s_lshl_b32 s61, s50, 7\n"
    "s_add_u32 s61, s61, 4096\n"
    "v_and_b32 v86, 31, v1\n"
    "v_lshlrev_b32 v87, 7, v86\n"
    "v_lshlrev_b32 v88, 2, v86\n"
    "v_add_u32 v74, 57344, v72\n"
    "v_add_u32 v82, 114688, v72\n"
    "v_add_u32 v75, 57344, v73\n"
    "v_add_u32 v83, 114688, v73\n"
    "v_mov_b32 v89, 163792\n"
    "v_and_b32 v94, 7, v1\n"
    "v_lshlrev_b32 v94, 2, v94\n"
    "v_mov_b32 v95, 48059\n"
    "v_lshrrev_b32 v95, v94, v95\n"
    "v_and_b32 v95, 15, v95\n"
    "v_add_u32 v94, 163808, v94\n"
    "s_cmp_eq_u32 s40, 0\n"
    "s_cbranch_scc1 .Lk_poll\n"
    "s_mov_b32 s69, 4194304\n"
    ".Lk_l7:\n"
    "ds_read_b32 v85, v3\n"
    "s_waitcnt lgkmcnt(0)\n"
    "v_cmp_gt_u32 vcc, 10, v85\n"
    "s_and_b64 vcc, exec, vcc\n"
    "s_cbranch_vccz .Lk_l8\n"
    "s_sleep 1\n"
    "s_sub_u32 s69, s69, 1\n"
    "s_cbranch_scc0 .Lk_l7\n"
    ".Lk_l8:\n"
    ".Lk_l9:\n"
    "s_mov_b32 m0, s29\n"
    "s_nop 0\n"
    "buffer_load_dwordx4 v234, s[24:27], s28 offen lds\n"
    "buffer_load_dwordx4 v235, s[24:27], s28 offen offset:1024 lds\n"
    "buffer_load_dwordx4 v236, s[24:27], s28 offen offset:2048 lds\n"
    "buffer_load_dwordx4 v237, s[24:27], s28 offen offset:3072 lds\n"
    "s_add_u32 s28, s28, 98304\n"
    "s_add_u32 s29, s29, 12288\n"
    "s_sub_u32 s62, s62, 1\n"
    "s_cbranch_scc0 .Lk_l9\n"
    "s_lshl_b32 s50, s40, 12\n"
    "s_add_u32 s51, s50, 64\n"
    "v_add_u32 v56, s51, v4\n"
    "s_xor_b32 s51, s50, 8192\n"
    "s_add_u32 s51, s51, 64\n"
    "v_add_u32 v57, s51, v4\n"
    "v_lshrrev_b32 v246, 5, v1\n"
    "v_bfe_u32 v247, v1, 2, 2\n"
    "v_lshl_add_u32 v247, v246, 4, v247\n"
    "v_lshlrev_b32 v247, 7, v247\n"
    "v_bfe_u32 v248, v1, 4, 1\n"
    "v_and_b32 v249, 3, v1\n"
    "v_lshl_add_u32 v247, v248, 4, v247\n"
    "v_and_b32 v248, 2, v1\n"
    "v_lshl_add_u32 v247, v248, 5, v247\n"
    "v_and_b32 v248, 1, v1\n"
    "v_lshl_add_u32 v247, v248, 3, v247\n"
    "s_lshl_b32 s51, s52, 12\n"
    "s_lshl_b32 s53, s41, 5\n"
    "s_add_u32 s51, s51, s53\n"
    "s_add_u32 s51, s51, 143424\n"
    "v_add_u32 v58, s51, v247\n"
    "s_lshr_b32 s50, s50, 1\n"
    "s_add_u32 s50, s50, 16448\n"
    "v_bfe_u32 v247, v1, 1, 2\n"
    "v_lshlrev_b32 v246, 1, v246\n"
    "v_xor_b32 v247, v247, v246\n"
    "v_lshlrev_b32 v247, 4, v247\n"
    "v_lshl_add_u32 v248, v86, 6, s50\n"
    "v_add_u32 v59, v248, v247\n"
    "v_xad_u32 v60, v247, 16, v248\n"
    "v_bfe_u32 v247, v1, 3, 2\n"
    "v_xor_b32 v247, v247, v249\n"
    "v_lshrrev_b32 v246, 2, v1\n"
    "v_lshl_add_u32 v248, v246, 6, s50\n"
    "v_lshl_add_u32 v61, v247, 4, v248\n"
    "v_lshlrev_b32 v249, 4, v249\n"
    "v_lshl_add_u32 v62, v246, 10, v249\n"
    "v_add_u32 v63, 16384, v62\n"
    "s_lshl1_add_u32 s50, s46, s41\n"
    "s_lshl_b32 s50, s50, 15\n"
    "s_lshl1_add_u32 s51, s47, s52\n"
    "s_lshl_b32 s51, s51, 6\n"
    "s_add_u32 s64, s50, s51\n"
    "v_add_u32 v84, 16, v3\n"
    "s_mov_b32 s69, 4194304\n"
    ".Lk_l10:\n"
    "ds_read_b32 v85, v84\n"
    "s_waitcnt lgkmcnt(0)\n"
    "v_cmp_gt_u32 vcc, 2, v85\n"
    "s_and_b64 vcc, exec, vcc\n"
    "s_cbranch_vccz .Lk_l11\n"
    "s_sleep 1\n"
    "s_sub_u32 s69, s69, 1\n"
    "s_cbranch_scc0 .Lk_l10\n"
    ".Lk_l11:\n"
    "s_add_u32 m0, s29, 16384\n"
    "s_nop 0\n"
    "buffer_load_dwordx4 v234, s[24:27], s28 offen lds\n"
    "buffer_load_dwordx4 v235, s[24:27], s28 offen offset:1024 lds\n"
    "buffer_load_dwordx4 v236, s[24:27], s28 offen offset:2048 lds\n"
    "buffer_load_dwordx4 v237, s[24:27], s28 offen offset:3072 lds\n"
    "s_mov_b32 s69, 4194304\n"
    ".Lk_l12:\n"
    "ds_read_b32 v85, v89\n"
    "s_waitcnt lgkmcnt(0)\n"
    "v_cmp_gt_u32 vcc, 11, v85\n"
    "s_and_b64 vcc, exec, vcc\n"
    "s_cbranch_vccz .Lk_l13\n"
    "s_sub_u32 s69, s69, 1\n"
    "s_cbranch_scc0 .Lk_l12\n"
    ".Lk_l13:\n"
    "s_branch .Lk_go\n"
    ".Lk_poll:\n"
    "s_lshl_b32 s50, s40, 12\n"
    "s_add_u32 s51, s50, 64\n"
    "v_add_u32 v56, s51, v4\n"
    "s_xor_b32 s51, s50, 8192\n"
    "s_add_u32 s51, s51, 64\n"
    "v_add_u32 v57, s51, v4\n"
    "v_lshrrev_b32 v246, 5, v1\n"
    "v_bfe_u32 v247, v1, 2, 2\n"
    "v_lshl_add_u32 v247, v246, 4, v247\n"
    "v_lshlrev_b32 v247, 7, v247\n"
    "v_bfe_u32 v248, v1, 4, 1\n"
    "v_and_b32 v249, 3, v1\n"
    "v_lshl_add_u32 v247, v248, 4, v247\n"
    "v_and_b32 v248, 2, v1\n"
    "v_lshl_add_u32 v247, v248, 5, v247\n"
    "v_and_b32 v248, 1, v1\n"
    "v_lshl_add_u32 v247, v248, 3, v247\n"
    "s_lshl_b32 s51, s52, 12\n"
    "s_lshl_b32 s53, s41, 5\n"
    "s_add_u32 s51, s51, s53\n"
    "s_add_u32 s51, s51, 143424\n"
    "v_add_u32 v58, s51, v247\n"
    "s_lshr_b32 s50, s50, 1\n"
    "s_add_u32 s50, s50, 16448\n"
    "v_bfe_u32 v247, v1, 1, 2\n"
    "v_lshlrev_b32 v246, 1, v246\n"
    "v_xor_b32 v247, v247, v246\n"
    "v_lshlrev_b32 v247, 4, v247\n"
    "v_lshl_add_u32 v248, v86, 6, s50\n"
    "v_add_u32 v59, v248, v247\n"
    "v_xad_u32 v60, v247, 16, v248\n"
    "v_bfe_u32 v247, v1, 3, 2\n"
    "v_xor_b32 v247, v247, v249\n"
    "v_lshrrev_b32 v246, 2, v1\n"
    "v_lshl_add_u32 v248, v246, 6, s50\n"
    "v_lshl_add_u32 v61, v247, 4, v248\n"
    "v_lshlrev_b32 v249, 4, v249\n"
    "v_lshl_add_u32 v62, v246, 10, v249\n"
    "v_add_u32 v63, 16384, v62\n"
    "s_lshl1_add_u32 s50, s46, s41\n"
    "s_lshl_b32 s50, s50, 15\n"
    "s_lshl1_add_u32 s51, s47, s52\n"
    "s_lshl_b32 s51, s51, 6\n"
    "s_add_u32 s64, s50, s51\n"
    "v_add_u32 v84, 16, v3\n"
    "s_lshl_b32 s54, s46, 8\n"
    "s_add_u32 s54, s54, 81920\n"
    "s_sleep 2\n"
    "s_cmp_lt_u32 s45, 14\n"
    "s_cbranch_scc1 .Lk_pgo\n"
    "s_sleep 17\n"
    "s_cmp_lt_u32 s45, 15\n"
    "s_cbranch_scc1 .Lk_pgo\n"
    "s_sleep 50\n"
    ".Lk_pgo:\n"
    "s_mov_b32 s69, 65536\n"
    "s_mov_b32 exec_hi, 0\n"
    ".Lk_l14:\n"
    "v_mov_b32 v92, 0\n"
    "buffer_atomic_add v92, v88, s[12:15], s54 offen sc0\n"
    "s_waitcnt vmcnt(0)\n"
    "v_cmp_ne_u32 vcc, s66, v92\n"
    "s_and_b64 vcc, exec, vcc\n"
    "s_cbranch_vccz .Lk_l15\n"
    "s_sub_u32 s69, s69, 1\n"
    "s_cbranch_scc0 .Lk_l14\n"
    ".Lk_l15:\n"
    "s_mov_b32 exec_hi, -1\n"
    "v_mov_b32 v6, 11\n"
    "ds_write_b32 v89, v6\n"
    "s_waitcnt lgkmcnt(1)\n"
    ".Lk_go:\n"
    "buffer_load_dword v91, v88, s[12:15], s61 offen sc1\n"
    "buffer_load_dwordx4 v[124:127], v4, s[12:15], s60 offen sc1\n"
    "buffer_load_dwordx4 v[128:131], v4, s[12:15], s60 offen offset:2048 sc1\n"
    "s_add_u32 s60, s60, 4096\n"
    "buffer_load_dwordx4 v[132:135], v4, s[12:15], s60 offen sc1\n"
    "buffer_load_dwordx4 v[136:139], v4, s[12:15], s60 offen offset:2048 sc1\n"
    "s_add_u32 s60, s60, 4096\n"
    "buffer_load_dwordx4 v[144:147], v4, s[12:15], s60 offen sc1\n"
    "buffer_load_dwordx4 v[148:151], v4, s[12:15], s60 offen offset:2048 sc1\n"
    "s_add_u32 s60, s60, 4096\n"
    "buffer_load_dwordx4 v[152:155], v4, s[12:15], s60 offen sc1\n"
    "buffer_load_dwordx4 v[160:163], v4, s[12:15], s60 offen offset:2048 sc1\n"
    "s_add_u32 s60, s60, 4096\n"
    "buffer_load_dwordx4 v[164:167], v4, s[12:15], s60 offen sc1\n"
    "buffer_load_dwordx4 v[168:171], v4, s[12:15], s60 offen offset:2048 sc1\n"
    "s_add_u32 s60, s60, 4096\n"
    "buffer_load_dwordx4 v[176:179], v4, s[12:15], s60 offen sc1\n"
    "buffer_load_dwordx4 v[180:183], v4, s[12:15], s60 offen offset:2048 sc1\n"
    "s_add_u32 s60, s60, 4096\n"
    "buffer_load_dwordx4 v[184:187], v4, s[12:15], s60 offen sc1\n"
    ".p2align 3 \n"
    "s_waitcnt vmcnt(12)\n"
    "v_mfma_f32_32x32x16_bf16 v[240:255], v[12:15], v[124:127], 0\n"
    "v_mfma_f32_32x32x16_bf16 v[224:239], v[8:11], v[124:127], 0\n"
    "v_mov_b32 v6, 11\n"
    "ds_write_b32 v2, v6\n"
    "buffer_load_dwordx4 v[192:195], v4, s[12:15], s60 offen offset:2048 sc1\n"
    "s_add_u32 s60, s60, 4096\n"
    "s_waitcnt vmcnt(12)\n"
    "v_mfma_f32_32x32x16_bf16 v[240:255], v[20:23], v[128:131], v[240:255]\n"
    "v_mfma_f32_32x32x16_bf16 v[224:239], v[16:19], v[128:131], v[224:239]\n"
    "buffer_load_dwordx4 v[196:199], v4, s[12:15], s60 offen sc1\n"
    "s_waitcnt vmcnt(12)\n"
    "v_mfma_f32_32x32x16_bf16 v[240:255], v[28:31], v[132:135], v[240:255]\n"
    "v_mfma_f32_32x32x16_bf16 v[224:239], v[24:27], v[132:135], v[224:239]\n"
    "buffer_load_dwordx4 v[200:203], v4, s[12:15], s60 offen offset:2048 sc1\n"
    "s_add_u32 s60, s60, 4096\n"
    "s_waitcnt vmcnt(12)\n"
    "v_mfma_f32_32x32x16_bf16 v[240:255], v[36:39], v[136:139], v[240:255]\n"
    "v_mfma_f32_32x32x16_bf16 v[224:239], v[32:35], v[136:139], v[224:239]\n"
    "buffer_load_dwordx4 v[208:211], v4, s[12:15], s60 offen sc1\n"
    "s_waitcnt vmcnt(12)\n"
    "v_mfma_f32_32x32x16_bf16 v[240:255], v[44:47], v[144:147], v[240:255]\n"
    "v_mfma_f32_32x32x16_bf16 v[224:239], v[40:43], v[144:147], v[224:239]\n"
    "buffer_load_dwordx4 v[212:215], v4, s[12:15], s60 offen offset:2048 sc1\n"
    "s_add_u32 s60, s60, 4096\n"
    "s_waitcnt vmcnt(12)\n"
    "v_mfma_f32_32x32x16_bf16 v[240:255], v[52:55], v[148:151], v[240:255]\n"
    "v_mfma_f32_32x32x16_bf16 v[224:239], v[48:51], v[148:151], v[224:239]\n"
    "buffer_load_dwordx4 v[216:219], v4, s[12:15], s60 offen sc1\n"
    "s_waitcnt vmcnt(12)\n"
    "v_mfma_f32_32x32x16_bf16 v[240:255], v[68:71], v[152:155], v[240:255]\n"
    "ds_read_b64_tr_b16 v[120:121], v73\n"
    "ds_read_b64_tr_b16 v[122:123], v73 offset:512\n"
    "v_mfma_f32_32x32x16_bf16 v[224:239], v[64:67], v[152:155], v[224:239]\n"
    "ds_read_b64_tr_b16 v[116:117], v72\n"
    "ds_read_b64_tr_b16 v[118:119], v72 offset:512\n"
    "buffer_load_dwordx4 v[140:143], v4, s[12:15], s60 offen offset:2048 sc1\n"
    "s_add_u32 s60, s60, 4096\n"
    "s_waitcnt vmcnt(12)\n"
    "v_mfma_f32_32x32x16_bf16 v[240:255], v[96:99], v[160:163], v[240:255]\n"
    "ds_read_b64_tr_b16 v[104:105], v73 offset:4096\n"
    "ds_read_b64_tr_b16 v[106:107], v73 offset:4608\n"
    "v_mfma_f32_32x32x16_bf16 v[224:239], v[76:79], v[160:163], v[224:239]\n"
    "ds_read_b64_tr_b16 v[100:101], v72 offset:4096\n"
    "ds_read_b64_tr_b16 v[102:103], v72 offset:4608\n"
    "buffer_load_dwordx4 v[156:159], v4, s[12:15], s60 offen sc1\n"
    "s_waitcnt vmcnt(12) lgkmcnt(6)\n"
    "v_mfma_f32_32x32x16_bf16 v[240:255], v[120:123], v[164:167], v[240:255]\n"
    "ds_read_b64_tr_b16 v[112:113], v73 offset:8192\n"
    "ds_read_b64_tr_b16 v[114:115], v73 offset:8704\n"
    "s_waitcnt lgkmcnt(6)\n"
    "v_mfma_f32_32x32x16_bf16 v[224:239], v[116:119], v[164:167], v[224:239]\n"
    "ds_read_b64_tr_b16 v[108:109], v72 offset:8192\n"
    "ds_read_b64_tr_b16 v[110:111], v72 offset:8704\n"
    "buffer_load_dwordx4 v[172:175], v4, s[12:15], s60 offen offset:2048 sc1\n"
    "s_add_u32 s60, s60, 4096\n"
    "s_waitcnt vmcnt(12) lgkmcnt(6)\n"
    "v_mfma_f32_32x32x16_bf16 v[240:255], v[104:107], v[168:171], v[240:255]\n"
    "ds_read_b64_tr_b16 v[120:121], v73 offset:12288\n"
    "ds_read_b64_tr_b16 v[122:123], v73 offset:12800\n"
    "s_waitcnt lgkmcnt(6)\n"
    "v_mfma_f32_32x32x16_bf16 v[224:239], v[100:103], v[168:171], v[224:239]\n"
    "ds_read_b64_tr_b16 v[116:117], v72 offset:12288\n"
    "ds_read_b64_tr_b16 v[118:119], v72 offset:12800\n"
    "buffer_load_dwordx4 v[188:191], v4, s[12:15], s60 offen sc1\n"
    "s_waitcnt vmcnt(12) lgkmcnt(6)\n"
    "v_mfma_f32_32x32x16_bf16 v[240:255], v[112:115], v[176:179], v[240:255]\n"
    "ds_read_b64_tr_b16 v[104:105], v73 offset:16384\n"
    "ds_read_b64_tr_b16 v[106:107], v73 offset:16896\n"
    "s_waitcnt lgkmcnt(6)\n"
    "v_mfma_f32_32x32x16_bf16 v[224:239], v[108:111], v[176:179], v[224:239]\n"
    "ds_read_b64_tr_b16 v[100:101], v72 offset:16384\n"
    "ds_read_b64_tr_b16 v[102:103], v72 offset:16896\n"
    "ds_read_b32 v85, v94\n"
    "buffer_load_dwordx4 v[204:207], v4, s[12:15], s60 offen offset:2048 sc1\n"
    "s_add_u32 s60, s60, 4096\n"
    "s_waitcnt vmcnt(12) lgkmcnt(7)\n"
    "v_mfma_f32_32x32x16_bf16 v[240:255], v[120:123], v[180:183], v[240:255]\n"
    "ds_read_b64_tr_b16 v[112:113], v73 offset:20480\n"
    "ds_read_b64_tr_b16 v[114:115], v73 offset:20992\n"
    "s_waitcnt lgkmcnt(7)\n"
    "v_mfma_f32_32x32x16_bf16 v[224:239], v[116:119], v[180:183], v[224:239]\n"
    "ds_read_b64_tr_b16 v[108:109], v72 offset:20480\n"
    "ds_read_b64_tr_b16 v[110:111], v72 offset:20992\n"
    "buffer_load_dwordx4 v[220:223], v4, s[12:15], s60 offen sc1\n"
    "s_waitcnt vmcnt(12) lgkmcnt(7)\n"
    "v_mfma_f32_32x32x16_bf16 v[240:255], v[104:107], v[184:187], v[240:255]\n"
    "ds_read_b64_tr_b16 v[120:121], v73 offset:24576\n"
    "ds_read_b64_tr_b16 v[122:123], v73 offset:25088\n"
    "s_waitcnt lgkmcnt(7)\n"
    "v_mfma_f32_32x32x16_bf16 v[224:239], v[100:103], v[184:187], v[224:239]\n"
    "ds_read_b64_tr_b16 v[116:117], v72 offset:24576\n"
    "ds_read_b64_tr_b16 v[118:119], v72 offset:25088\n"
    "s_waitcnt lgkmcnt(8)\n"
    "v_cmp_gt_u32 vcc, v95, v85\n"
    "s_and_b64 vcc, exec, vcc\n"
    "s_cbranch_vccz .Lk_wok\n"
    "s_mov_b32 s69, 4194304\n"
    ".Lk_l16:\n"
    "ds_read_b32 v85, v94\n"
    "s_waitcnt lgkmcnt(0)\n"
    "v_cmp_gt_u32 vcc, v95, v85\n"
    "s_and_b64 vcc, exec, vcc\n"
    "s_cbranch_vccz .Lk_l17\n"
    "s_sleep 1\n"
    "s_sub_u32 s69, s69, 1\n"
    "s_cbranch_scc0 .Lk_l16\n"
    ".Lk_l17:\n"
    ".Lk_wok:\n"
    "s_add_u32 s60, s60, s63\n"
    "buffer_load_dwordx4 v[124:127], v4, s[12:15], s60 offen sc1\n"
    "s_waitcnt vmcnt(12)\n"
    "v_mfma_f32_32x32x16_bf16 v[240:255], v[112:115], v[192:195], v[240:255]\n"
    "ds_read_b64_tr_b16 v[104:105], v73 offset:28672\n"
    "ds_read_b64_tr_b16 v[106:107], v73 offset:29184\n"
    "v_mfma_f32_32x32x16_bf16 v[224:239], v[108:111], v[192:195], v[224:239]\n"
    "ds_read_b64_tr_b16 v[100:101], v72 offset:28672\n"
    "ds_read_b64_tr_b16 v[102:103], v72 offset:29184\n"
    "buffer_load_dwordx4 v[8:11], v4, s[12:15], s60 offen offset:2048 sc1\n"
    "s_add_u32 s60, s60, 4096\n"
    "s_waitcnt vmcnt(12)\n"
    "v_mfma_f32_32x32x16_bf16 v[240:255], v[120:123], v[196:199], v[240:255]\n"
    "ds_read_b64_tr_b16 v[112:113], v73 offset:32768\n"
    "ds_read_b64_tr_b16 v[114:115], v73 offset:33280\n"
    "v_mfma_f32_32x32x16_bf16 v[224:239], v[116:119], v[196:199], v[224:239]\n"
    "ds_read_b64_tr_b16 v[108:109], v72 offset:32768\n"
    "ds_read_b64_tr_b16 v[110:111], v72 offset:33280\n"
    "buffer_load_dwordx4 v[12:15], v4, s[12:15], s60 offen sc1\n"
    "s_waitcnt vmcnt(12) lgkmcnt(6)\n"
    "v_mfma_f32_32x32x16_bf16 v[240:255], v[104:107], v[200:203], v[240:255]\n"
    "ds_read_b64_tr_b16 v[120:121], v83\n"
    "ds_read_b64_tr_b16 v[122:123], v83 offset:512\n"
    "s_waitcnt lgkmcnt(6)\n"
    "v_mfma_f32_32x32x16_bf16 v[224:239], v[100:103], v[200:203], v[224:239]\n"
    "ds_read_b64_tr_b16 v[116:117], v82\n"
    "ds_read_b64_tr_b16 v[118:119], v82 offset:512\n"
    "buffer_load_dwordx4 v[128:131], v4, s[12:15], s60 offen offset:2048 sc1\n"
    "s_add_u32 s60, s60, 4096\n"
    "s_waitcnt vmcnt(12) lgkmcnt(6)\n"
    "v_mfma_f32_32x32x16_bf16 v[240:255], v[112:115], v[208:211], v[240:255]\n"
    "ds_read_b64_tr_b16 v[104:105], v83 offset:4096\n"
    "ds_read_b64_tr_b16 v[106:107], v83 offset:4608\n"
    "s_waitcnt lgkmcnt(6)\n"
    "v_mfma_f32_32x32x16_bf16 v[224:239], v[108:111], v[208:211], v[224:239]\n"
    "ds_read_b64_tr_b16 v[100:101], v82 offset:4096\n"
    "ds_read_b64_tr_b16 v[102:103], v82 offset:4608\n"
    "v_mov_b32 v6, 12\n"
    "ds_write_b32 v2, v6\n"
    "buffer_load_dwordx4 v[16:19], v4, s[12:15], s60 offen sc1\n"
    "s_waitcnt vmcnt(12) lgkmcnt(7)\n"
    "v_mfma_f32_32x32x16_bf16 v[240:255], v[120:123], v[212:215], v[240:255]\n"
    "ds_read_b64_tr_b16 v[112:113], v83 offset:8192\n"
    "ds_read_b64_tr_b16 v[114:115], v83 offset:8704\n"
    "s_waitcnt lgkmcnt(7)\n"
    "v_mfma_f32_32x32x16_bf16 v[224:239], v[116:119], v[212:215], v[224:239]\n"
    "ds_read_b64_tr_b16 v[108:109], v82 offset:8192\n"
    "ds_read_b64_tr_b16 v[110:111], v82 offset:8704\n"
    "buffer_load_dwordx4 v[20:23], v4, s[12:15], s60 offen offset:2048 sc1\n"
    "s_add_u32 s60, s60, 4096\n"
    "s_waitcnt vmcnt(12) lgkmcnt(7)\n"
    "v_mfma_f32_32x32x16_bf16 v[240:255], v[104:107], v[216:219], v[240:255]\n"
    "ds_read_b64_tr_b16 v[120:121], v83 offset:12288\n"
    "ds_read_b64_tr_b16 v[122:123], v83 offset:12800\n"
    "s_waitcnt lgkmcnt(7)\n"
    "v_mfma_f32_32x32x16_bf16 v[224:239], v[100:103], v[216:219], v[224:239]\n"
    "ds_read_b64_tr_b16 v[116:117], v82 offset:12288\n"
    "ds_read_b64_tr_b16 v[118:119], v82 offset:12800\n"
    "buffer_load_dwordx4 v[132:135], v4, s[12:15], s60 offen sc1\n"
    "s_waitcnt vmcnt(12) lgkmcnt(6)\n"
    "v_mfma_f32_32x32x16_bf16 v[240:255], v[112:115], v[140:143], v[240:255]\n"
    "ds_read_b64_tr_b16 v[104:105], v73 offset:36864\n"
    "ds_read_b64_tr_b16 v[106:107], v73 offset:37376\n"
    "s_waitcnt lgkmcnt(6)\n"
    "v_mfma_f32_32x32x16_bf16 v[224:239], v[108:111], v[140:143], v[224:239]\n"
    "ds_read_b64_tr_b16 v[100:101], v72 offset:36864\n"
    "ds_read_b64_tr_b16 v[102:103], v72 offset:37376\n"
    "buffer_load_dwordx4 v[24:27], v4, s[12:15], s60 offen offset:2048 sc1\n"
    "s_add_u32 s60, s60, 4096\n"
    "s_waitcnt vmcnt(12) lgkmcnt(6)\n"
    "v_mfma_f32_32x32x16_bf16 v[240:255], v[120:123], v[156:159], v[240:255]\n"
    "ds_read_b64_tr_b16 v[112:113], v73 offset:40960\n"
    "ds_read_b64_tr_b16 v[114:115], v73 offset:41472\n"
    "s_waitcnt lgkmcnt(6)\n"
    "v_mfma_f32_32x32x16_bf16 v[224:239], v[116:119], v[156:159], v[224:239]\n"
    "ds_read_b64_tr_b16 v[108:109], v72 offset:40960\n"
    "ds_read_b64_tr_b16 v[110:111], v72 offset:41472\n"
    "buffer_load_dwordx4 v[28:31], v4, s[12:15], s60 offen sc1\n"
    "s_waitcnt vmcnt(12) lgkmcnt(6)\n"
    "v_mfma_f32_32x32x16_bf16 v[240:255], v[104:107], v[172:175], v[240:255]\n"
    "ds_read_b64_tr_b16 v[120:121], v73 offset:45056\n"
    "ds_read_b64_tr_b16 v[122:123], v73 offset:45568\n"
    "s_waitcnt lgkmcnt(6)\n"
    "v_mfma_f32_32x32x16_bf16 v[224:239], v[100:103], v[172:175], v[224:239]\n"
    "ds_read_b64_tr_b16 v[116:117], v72 offset:45056\n"
    "ds_read_b64_tr_b16 v[118:119], v72 offset:45568\n"
    "buffer_load_dwordx4 v[136:139], v4, s[12:15], s60 offen offset:2048 sc1\n"
    "s_add_u32 s60, s60, 4096\n"
    "s_waitcnt vmcnt(12) lgkmcnt(6)\n"
    "v_mfma_f32_32x32x16_bf16 v[240:255], v[112:115], v[188:191], v[240:255]\n"
    "ds_read_b64_tr_b16 v[104:105], v73 offset:49152\n"
    "ds_read_b64_tr_b16 v[106:107], v73 offset:49664\n"
    "s_waitcnt lgkmcnt(6)\n"
    "v_mfma_f32_32x32x16_bf16 v[224:239], v[108:111], v[188:191], v[224:239]\n"
    "ds_read_b64_tr_b16 v[100:101], v72 offset:49152\n"
    "ds_read_b64_tr_b16 v[102:103], v72 offset:49664\n"
    "buffer_load_dwordx4 v[32:35], v4, s[12:15], s60 offen sc1\n"
    "s_waitcnt vmcnt(12) lgkmcnt(6)\n"
    "v_mfma_f32_32x32x16_bf16 v[240:255], v[120:123], v[204:207], v[240:255]\n"
    "ds_read_b64_tr_b16 v[112:113], v73 offset:53248\n"
    "ds_read_b64_tr_b16 v[114:115], v73 offset:53760\n"
    "s_waitcnt lgkmcnt(6)\n"
    "v_mfma_f32_32x32x16_bf16 v[224:239], v[116:119], v[204:207], v[224:239]\n"
    "ds_read_b64_tr_b16 v[108:109], v72 offset:53248\n"
    "ds_read_b64_tr_b16 v[110:111], v72 offset:53760\n"
    "buffer_load_dwordx4 v[36:39], v4, s[12:15], s60 offen offset:2048 sc1\n"
    "s_add_u32 s60, s60, 4096\n"
    "s_waitcnt vmcnt(12) lgkmcnt(6)\n"
    "v_mfma_f32_32x32x16_bf16 v[240:255], v[104:107], v[220:223], v[240:255]\n"
    "ds_read_b64_tr_b16 v[120:121], v75\n"
    "ds_read_b64_tr_b16 v[122:123], v75 offset:512\n"
    "s_waitcnt lgkmcnt(6)\n"
    "v_mfma_f32_32x32x16_bf16 v[224:239], v[100:103], v[220:223], v[224:239]\n"
    "ds_read_b64_tr_b16 v[116:117], v74\n"
    "ds_read_b64_tr_b16 v[118:119], v74 offset:512\n"
    "buffer_load_dwordx4 v[144:147], v4, s[12:15], s60 offen sc1\n"
    "s_waitcnt vmcnt(12) lgkmcnt(6)\n"
    "v_mfma_f32_32x32x16_bf16 v[240:255], v[112:115], v[124:127], v[240:255]\n"
    "ds_read_b64_tr_b16 v[104:105], v75 offset:4096\n"
    "ds_read_b64_tr_b16 v[106:107], v75 offset:4608\n"
    "s_waitcnt lgkmcnt(6)\n"
    "v_mfma_f32_32x32x16_bf16 v[224:239], v[108:111], v[124:127], v[224:239]\n"
    "ds_read_b64_tr_b16 v[100:101], v74 offset:4096\n"
    "ds_read_b64_tr_b16 v[102:103], v74 offset:4608\n"
    "buffer_load_dwordx4 v[40:43], v4, s[12:15], s60 offen offset:2048 sc1\n"
    "s_add_u32 s60, s60, 4096\n"
    "s_waitcnt vmcnt(12) lgkmcnt(6)\n"
    "v_mfma_f32_32x32x16_bf16 v[240:255], v[120:123], v[8:11], v[240:255]\n"
    "ds_read_b64_tr_b16 v[112:113], v75 offset:8192\n"
    "ds_read_b64_tr_b16 v[114:115], v75 offset:8704\n"
    "s_waitcnt lgkmcnt(6)\n"
    "v_mfma_f32_32x32x16_bf16 v[224:239], v[116:119], v[8:11], v[224:239]\n"
    "ds_read_b64_tr_b16 v[108:109], v74 offset:8192\n"
    "ds_read_b64_tr_b16 v[110:111], v74 offset:8704\n"
    "buffer_load_dwordx4 v[44:47], v4, s[12:15], s60 offen sc1\n"
    "s_waitcnt vmcnt(12) lgkmcnt(6)\n"
    "v_mfma_f32_32x32x16_bf16 v[240:255], v[104:107], v[12:15], v[240:255]\n"
    "ds_read_b64_tr_b16 v[120:121], v75 offset:12288\n"
    "ds_read_b64_tr_b16 v[122:123], v75 offset:12800\n"
    "s_waitcnt lgkmcnt(6)\n"
    "v_mfma_f32_32x32x16_bf16 v[224:239], v[100:103], v[12:15], v[224:239]\n"
    "ds_read_b64_tr_b16 v[116:117], v74 offset:12288\n"
    "ds_read_b64_tr_b16 v[118:119], v74 offset:12800\n"
    "buffer_load_dwordx4 v[148:151], v4, s[12:15], s60 offen offset:2048 sc1\n"
    "s_add_u32 s60, s60, 4096\n"
    "s_waitcnt vmcnt(12) lgkmcnt(6)\n"
    "v_mfma_f32_32x32x16_bf16 v[240:255], v[112:115], v[128:131], v[240:255]\n"
    "ds_read_b64_tr_b16 v[104:105], v75 offset:16384\n"
    "ds_read_b64_tr_b16 v[106:107], v75 offset:16896\n"
    "s_waitcnt lgkmcnt(6)\n"
    "v_mfma_f32_32x32x16_bf16 v[224:239], v[108:111], v[128:131], v[224:239]\n"
    "ds_read_b64_tr_b16 v[100:101], v74 offset:16384\n"
    "ds_read_b64_tr_b16 v[102:103], v74 offset:16896\n"
    "buffer_load_dwordx4 v[48:51], v4, s[12:15], s60 offen sc1\n"
    "s_waitcnt vmcnt(12) lgkmcnt(6)\n"
    "v_mfma_f32_32x32x16_bf16 v[240:255], v[120:123], v[16:19], v[240:255]\n"
    "ds_read_b64_tr_b16 v[112:113], v75 offset:20480\n"
    "ds_read_b64_tr_b16 v[114:115], v75 offset:20992\n"
    "s_waitcnt lgkmcnt(6)\n"
    "v_mfma_f32_32x32x16_bf16 v[224:239], v[116:119], v[16:19], v[224:239]\n"
    "ds_read_b64_tr_b16 v[108:109], v74 offset:20480\n"
    "ds_read_b64_tr_b16 v[110:111], v74 offset:20992\n"
    "buffer_load_dwordx4 v[52:55], v4, s[12:15], s60 offen offset:2048 sc1\n"
    "s_add_u32 s60, s60, 4096\n"
    "s_waitcnt vmcnt(12) lgkmcnt(6)\n"
    "v_mfma_f32_32x32x16_bf16 v[240:255], v[104:107], v[20:23], v[240:255]\n"
    "ds_read_b64_tr_b16 v[120:121], v75 offset:24576\n"
    "ds_read_b64_tr_b16 v[122:123], v75 offset:25088\n"
    "s_waitcnt lgkmcnt(6)\n"
    "v_mfma_f32_32x32x16_bf16 v[224:239], v[100:103], v[20:23], v[224:239]\n"
    "ds_read_b64_tr_b16 v[116:117], v74 offset:24576\n"
    "ds_read_b64_tr_b16 v[118:119], v74 offset:25088\n"
    "s_waitcnt vmcnt(11) lgkmcnt(6)\n"
    "v_mfma_f32_32x32x16_bf16 v[240:255], v[112:115], v[132:135], v[240:255]\n"
    "ds_read_b64_tr_b16 v[104:105], v75 offset:28672\n"
    "ds_read_b64_tr_b16 v[106:107], v75 offset:29184\n"
    "s_waitcnt lgkmcnt(6)\n"
    "v_mfma_f32_32x32x16_bf16 v[224:239], v[108:111], v[132:135], v[224:239]\n"
    "ds_read_b64_tr_b16 v[100:101], v74 offset:28672\n"
    "ds_read_b64_tr_b16 v[102:103], v74 offset:29184\n"
    "s_waitcnt vmcnt(10) lgkmcnt(6)\n"
    "v_mfma_f32_32x32x16_bf16 v[240:255], v[120:123], v[24:27], v[240:255]\n"
    "ds_read_b64_tr_b16 v[112:113], v75 offset:32768\n"
    "ds_read_b64_tr_b16 v[114:115], v75 offset:33280\n"
    "s_waitcnt lgkmcnt(6)\n"
    "v_mfma_f32_32x32x16_bf16 v[224:239], v[116:119], v[24:27], v[224:239]\n"
    "ds_read_b64_tr_b16 v[108:109], v74 offset:32768\n"
    "ds_read_b64_tr_b16 v[110:111], v74 offset:33280\n"
    "s_waitcnt vmcnt(9) lgkmcnt(6)\n"
    "v_mfma_f32_32x32x16_bf16 v[240:255], v[104:107], v[28:31], v[240:255]\n"
    "ds_read_b64_tr_b16 v[120:121], v75 offset:36864\n"
    "ds_read_b64_tr_b16 v[122:123], v75 offset:37376\n"
    "s_waitcnt lgkmcnt(6)\n"
    "v_mfma_f32_32x32x16_bf16 v[224:239], v[100:103], v[28:31], v[224:239]\n"
    "ds_read_b64_tr_b16 v[116:117], v74 offset:36864\n"
    "ds_read_b64_tr_b16 v[118:119], v74 offset:37376\n"
    "s_waitcnt vmcnt(8) lgkmcnt(6)\n"
    "v_mfma_f32_32x32x16_bf16 v[240:255], v[112:115], v[136:139], v[240:255]\n"
    "ds_read_b64_tr_b16 v[104:105], v75 offset:40960\n"
    "ds_read_b64_tr_b16 v[106:107], v75 offset:41472\n"
    "s_waitcnt lgkmcnt(6)\n"
    "v_mfma_f32_32x32x16_bf16 v[224:239], v[108:111], v[136:139], v[224:239]\n"
    "ds_read_b64_tr_b16 v[100:101], v74 offset:40960\n"
    "ds_read_b64_tr_b16 v[102:103], v74 offset:41472\n"
    "s_waitcnt vmcnt(7) lgkmcnt(6)\n"
    "v_mfma_f32_32x32x16_bf16 v[240:255], v[120:123], v[32:35], v[240:255]\n"
    "ds_read_b64_tr_b16 v[112:113], v75 offset:45056\n"
    "ds_read_b64_tr_b16 v[114:115], v75 offset:45568\n"
    "s_waitcnt lgkmcnt(6)\n"
    "v_mfma_f32_32x32x16_bf16 v[224:239], v[116:119], v[32:35], v[224:239]\n"
    "ds_read_b64_tr_b16 v[108:109], v74 offset:45056\n"
    "ds_read_b64_tr_b16 v[110:111], v74 offset:45568\n"
    "s_waitcnt vmcnt(6) lgkmcnt(6)\n"
    "v_mfma_f32_32x32x16_bf16 v[240:255], v[104:107], v[36:39], v[240:255]\n"
    "ds_read_b64_tr_b16 v[120:121], v75 offset:49152\n"
    "ds_read_b64_tr_b16 v[122:123], v75 offset:49664\n"
    "s_waitcnt lgkmcnt(6)\n"
    "v_mfma_f32_32x32x16_bf16 v[224:239], v[100:103], v[36:39], v[224:239]\n"
    "ds_read_b64_tr_b16 v[116:117], v74 offset:49152\n"
    "ds_read_b64_tr_b16 v[118:119], v74 offset:49664\n"
    "s_waitcnt vmcnt(5) lgkmcnt(6)\n"
    "v_mfma_f32_32x32x16_bf16 v[240:255], v[112:115], v[144:147], v[240:255]\n"
    "ds_read_b64_tr_b16 v[104:105], v75 offset:53248\n"
    "ds_read_b64_tr_b16 v[106:107], v75 offset:53760\n"
    "s_waitcnt lgkmcnt(6)\n"
    "v_mfma_f32_32x32x16_bf16 v[224:239], v[108:111], v[144:147], v[224:239]\n"
    "ds_read_b64_tr_b16 v[100:101], v74 offset:53248\n"
    "ds_read_b64_tr_b16 v[102:103], v74 offset:53760\n"
    "ds_read_b32 v85, v84\n"
    "s_waitcnt vmcnt(4) lgkmcnt(7)\n"
    "v_mfma_f32_32x32x16_bf16 v[240:255], v[120:123], v[40:43], v[240:255]\n"
    "ds_read_b64_tr_b16 v[112:113], v83 offset:16384\n"
    "ds_read_b64_tr_b16 v[114:115], v83 offset:16896\n"
    "s_waitcnt lgkmcnt(7)\n"
    "v_mfma_f32_32x32x16_bf16 v[224:239], v[116:119], v[40:43], v[224:239]\n"
    "ds_read_b64_tr_b16 v[108:109], v82 offset:16384\n"
    "ds_read_b64_tr_b16 v[110:111], v82 offset:16896\n"
    "s_waitcnt vmcnt(3) lgkmcnt(7)\n"
    "v_mfma_f32_32x32x16_bf16 v[240:255], v[104:107], v[44:47], v[240:255]\n"
    "ds_read_b64_tr_b16 v[120:121], v83 offset:20480\n"
    "ds_read_b64_tr_b16 v[122:123], v83 offset:20992\n"
    "s_waitcnt lgkmcnt(7)\n"
    "v_mfma_f32_32x32x16_bf16 v[224:239], v[100:103], v[44:47], v[224:239]\n"
    "ds_read_b64_tr_b16 v[116:117], v82 offset:20480\n"
    "ds_read_b64_tr_b16 v[118:119], v82 offset:20992\n"
    "s_waitcnt lgkmcnt(8)\n"
    "v_cmp_gt_u32 vcc, 5, v85\n"
    "s_and_b64 vcc, exec, vcc\n"
    "s_cbranch_vccz .Lk_aok2\n"
    "s_mov_b32 s69, 4194304\n"
    ".Lk_l18:\n"
    "ds_read_b32 v85, v84\n"
    "s_waitcnt lgkmcnt(0)\n"
    "v_cmp_gt_u32 vcc, 5, v85\n"
    "s_and_b64 vcc, exec, vcc\n"
    "s_cbranch_vccz .Lk_l19\n"
    "s_sub_u32 s69, s69, 1\n"
    "s_cbranch_scc0 .Lk_l18\n"
    ".Lk_l19:\n"
    ".Lk_aok2:\n"
    "ds_read_b64_tr_b16 v[152:153], v58\n"
    "ds_read_b64_tr_b16 v[154:155], v58 offset:512\n"
    "ds_read_b64_tr_b16 v[64:65], v58 offset:1024\n"
    "ds_read_b64_tr_b16 v[66:67], v58 offset:1536\n"
    "v_sub_f32 v90, 1.0, v91\n"
    "v_mul_f32 v91, 0x5f800000, v91\n"
    "s_waitcnt vmcnt(2) lgkmcnt(10)\n"
    "v_mfma_f32_32x32x16_bf16 v[240:255], v[112:115], v[148:151], v[240:255]\n"
    "ds_read_b64_tr_b16 v[104:105], v83 offset:24576\n"
    "ds_read_b64_tr_b16 v[106:107], v83 offset:25088\n"
    "s_waitcnt lgkmcnt(10)\n"
    "v_mfma_f32_32x32x16_bf16 v[224:239], v[108:111], v[148:151], v[224:239]\n"
    "ds_read_b64_tr_b16 v[100:101], v82 offset:24576\n"
    "ds_read_b64_tr_b16 v[102:103], v82 offset:25088\n"
    "s_waitcnt vmcnt(1) lgkmcnt(10)\n"
    "v_mfma_f32_32x32x16_bf16 v[240:255], v[120:123], v[48:51], v[240:255]\n"
    "s_waitcnt lgkmcnt(7)\n"
    "v_lshlrev_b32 v68, 16, v152\n"
    "v_and_b32 v69, 4294901760, v152\n"
    "v_lshlrev_b32 v70, 16, v153\n"
    "v_and_b32 v71, 4294901760, v153\n"
    "s_waitcnt lgkmcnt(6)\n"
    "v_lshlrev_b32 v160, 16, v154\n"
    "v_mfma_f32_32x32x16_bf16 v[224:239], v[116:119], v[48:51], v[224:239]\n"
    "v_and_b32 v161, 4294901760, v154\n"
    "v_lshlrev_b32 v162, 16, v155\n"
    "v_and_b32 v163, 4294901760, v155\n"
    "s_waitcnt lgkmcnt(5)\n"
    "v_lshlrev_b32 v76, 16, v64\n"
    "v_and_b32 v77, 4294901760, v64\n"
    "s_waitcnt vmcnt(0) lgkmcnt(2)\n"
    "v_mfma_f32_32x32x16_bf16 v[240:255], v[104:107], v[52:55], v[240:255]\n"
    "v_lshlrev_b32 v78, 16, v65\n"
    "v_and_b32 v79, 4294901760, v65\n"
    "v_lshlrev_b32 v96, 16, v66\n"
    "v_and_b32 v97, 4294901760, v66\n"
    "v_lshlrev_b32 v98, 16, v67\n"
    "s_waitcnt lgkmcnt(0)\n"
    "v_mfma_f32_32x32x16_bf16 v[224:239], v[100:103], v[52:55], v[224:239]\n"
    "v_and_b32 v99, 4294901760, v67\n"
    "s_nop 3\n"
    "ds_write_b128 v56, v[240:243] offset:0\n"
    "ds_write_b128 v56, v[244:247] offset:1024\n"
    "ds_write_b128 v56, v[248:251] offset:2048\n"
    "ds_write_b128 v56, v[252:255] offset:3072\n"
    "v_pk_mul_f32 v[68:69], v[68:69], v[90:91] op_sel:[0,1]\n"
    "v_pk_mul_f32 v[70:71], v[70:71], v[90:91] op_sel:[0,1]\n"
    "v_pk_mul_f32 v[160:161], v[160:161], v[90:91] op_sel:[0,1]\n"
    "v_pk_mul_f32 v[162:163], v[162:163], v[90:91] op_sel:[0,1]\n"
    "v_pk_mul_f32 v[76:77], v[76:77], v[90:91] op_sel:[0,1]\n"
    "v_pk_mul_f32 v[78:79], v[78:79], v[90:91] op_sel:[0,1]\n"
    "v_pk_mul_f32 v[96:97], v[96:97], v[90:91] op_sel:[0,1]\n"
    "v_pk_mul_f32 v[98:99], v[98:99], v[90:91] op_sel:[0,1]\n"
    "v_pk_fma_f32 v[224:225], v[224:225], v[90:91], v[68:69] op_sel_hi:[1,0,1]\n"
    "v_pk_fma_f32 v[226:227], v[226:227], v[90:91], v[70:71] op_sel_hi:[1,0,1]\n"
    "v_pk_fma_f32 v[228:229], v[228:229], v[90:91], v[160:161] op_sel_hi:[1,0,1]\n"
    "v_pk_fma_f32 v[230:231], v[230:231], v[90:91], v[162:163] op_sel_hi:[1,0,1]\n"
    "v_pk_fma_f32 v[232:233], v[232:233], v[90:91], v[76:77] op_sel_hi:[1,0,1]\n"
    "v_pk_fma_f32 v[234:235], v[234:235], v[90:91], v[78:79] op_sel_hi:[1,0,1]\n"
    "v_pk_fma_f32 v[236:237], v[236:237], v[90:91], v[96:97] op_sel_hi:[1,0,1]\n"
    "v_pk_fma_f32 v[238:239], v[238:239], v[90:91], v[98:99] op_sel_hi:[1,0,1]\n"
    "s_waitcnt lgkmcnt(0)\n"
    "s_barrier\n"
    "ds_read_b128 v[240:243], v57\n"
    "ds_read_b128 v[244:247], v57 offset:1024\n"
    "ds_read_b128 v[248:251], v57 offset:2048\n"
    "ds_read_b128 v[252:255], v57 offset:3072\n"
    "s_waitcnt lgkmcnt(3)\n"
    "v_pk_fma_f32 v[240:241], v[240:241], v[90:91], v[224:225] op_sel_hi:[1,0,1]\n"
    "v_pk_fma_f32 v[242:243], v[242:243], v[90:91], v[226:227] op_sel_hi:[1,0,1]\n"
    "s_waitcnt lgkmcnt(2)\n"
    "v_pk_fma_f32 v[244:245], v[244:245], v[90:91], v[228:229] op_sel_hi:[1,0,1]\n"
    "v_pk_fma_f32 v[246:247], v[246:247], v[90:91], v[230:231] op_sel_hi:[1,0,1]\n"
    "s_waitcnt lgkmcnt(1)\n"
    "v_pk_fma_f32 v[248:249], v[248:249], v[90:91], v[232:233] op_sel_hi:[1,0,1]\n"
    "v_pk_fma_f32 v[250:251], v[250:251], v[90:91], v[234:235] op_sel_hi:[1,0,1]\n"
    "s_waitcnt lgkmcnt(0)\n"
    "v_pk_fma_f32 v[252:253], v[252:253], v[90:91], v[236:237] op_sel_hi:[1,0,1]\n"
    "v_pk_fma_f32 v[254:255], v[254:255], v[90:91], v[238:239] op_sel_hi:[1,0,1]\n"
    "v_cvt_pk_bf16_f32 v48, v240, v241\n"
    "v_cvt_pk_bf16_f32 v49, v242, v243\n"
    "v_cvt_pk_bf16_f32 v50, v244, v245\n"
    "v_cvt_pk_bf16_f32 v51, v246, v247\n"
    "ds_write_b128 v59, v[48:51]\n"
    "v_cvt_pk_bf16_f32 v52, v248, v249\n"
    "v_cvt_pk_bf16_f32 v53, v250, v251\n"
    "v_cvt_pk_bf16_f32 v54, v252, v253\n"
    "v_cvt_pk_bf16_f32 v55, v254, v255\n"
    "ds_write_b128 v60, v[52:55]\n"
    "ds_read_b128 v[224:227], v61\n"
    "ds_read_b128 v[228:231], v61 offset:1024\n"
    "s_waitcnt lgkmcnt(1)\n"
    "buffer_store_dwordx4 v[224:227], v62, s[16:19], s64 offen sc1\n"
    "s_waitcnt lgkmcnt(0)\n"
    "buffer_store_dwordx4 v[228:231], v63, s[16:19], s64 offen sc1\n"
    "s_endpgm\n"
    ".Lk_gates:\n"
    "v_lshrrev_b32 v5, 5, v1\n"
    "v_lshlrev_b32 v5, 4, v5\n"
    "s_mov_b32 s50, 0\n"
    "s_mov_b32 s51, 7\n"
    ".Lk_gt1:\n"
    "s_lshl_b32 s52, s48, 7\n"
    "s_add_u32 s52, s52, 57376\n"
    "v_add_u32 v6, s52, v5\n"
    "s_barrier\n"
    "ds_read_b128 v[8:11], v6\n"
    "ds_read_b128 v[12:15], v224\n"
    "ds_read_b128 v[16:19], v6 offset:32\n"
    "ds_read_b128 v[20:23], v225\n"
    "ds_read_b128 v[24:27], v6 offset:64\n"
    "ds_read_b128 v[28:31], v226\n"
    "ds_read_b128 v[32:35], v6 offset:96\n"
    "ds_read_b128 v[36:39], v227\n"
    "s_lshr_b32 s49, s49, 4\n"
    "s_and_b32 s48, s49, 7\n"
    "s_cmp_eq_u32 s50, 81984\n"
    "s_cselect_b32 s52, 122976, 0\n"
    "s_sub_u32 s52, 40992, s52\n"
    "s_add_u32 s50, s50, s52\n"
    "s_waitcnt lgkmcnt(6)\n"
    "v_mfma_f32_32x32x16_bf16 v[128:143], v[8:11], v[12:15], v[128:143]\n"
    "v_add_u32 v224, s52, v224\n"
    "s_waitcnt lgkmcnt(4)\n"
    "v_mfma_f32_32x32x16_bf16 v[128:143], v[16:19], v[20:23], v[128:143]\n"
    "v_add_u32 v225, s52, v225\n"
    "s_waitcnt lgkmcnt(2)\n"
    "v_mfma_f32_32x32x16_bf16 v[128:143], v[24:27], v[28:31], v[128:143]\n"
    "v_add_u32 v226, s52, v226\n"
    "s_waitcnt lgkmcnt(0)\n"
    "v_mfma_f32_32x32x16_bf16 v[128:143], v[32:35], v[36:39], v[128:143]\n"
    "v_add_u32 v227, s52, v227\n"
    "s_sub_u32 s51, s51, 1\n"
    "s_cbranch_scc0 .Lk_gt1\n"
    "v_mov_b32 v6, 8\n"
    "ds_write_b32 v239, v6\n"
    "v_and_b32 v7, 31, v1\n"
    "v_lshlrev_b32 v7, 2, v7\n"
    "s_lshl2_add_u32 s50, s44, s40\n"
    "s_lshl_b32 s50, s50, 7\n"
    "s_add_u32 s50, s50, 4096\n"
    "s_nop 1\n"
    "v_mul_f32 v8, 0xbfb8aa3b, v128\n"
    "v_exp_f32 v8, v8\n"
    "s_nop 0\n"
    "v_add_f32 v8, 1.0, v8\n"
    "v_rcp_f32 v8, v8\n"
    "s_nop 0\n"
    "buffer_store_dword v8, v7, s[12:15], s50 offen sc1\n"
    "v_mov_b32 v82, 163776\n"
    "ds_read_b64 v[80:81], v82\n"
    "s_branch .Lk_gtail\n"
    ".Lk_h67:\n"
    "s_waitcnt vmcnt(0)\n"
    "v_readfirstlane_b32 s50, v6\n"
    "v_readfirstlane_b32 s51, v7\n"
    "s_mul_i32 s67, s50, 1481765933\n"
    "s_mul_hi_u32 s52, s50, 1284865837\n"
    "s_mul_i32 s51, s51, 1284865837\n"
    "s_mul_i32 s66, s50, 1284865837\n"
    "s_add_u32 s67, s67, s52\n"
    "s_add_u32 s67, s67, s51\n"
    "s_add_u32 s66, s66, 4150755663\n"
    "s_addc_u32 s67, s67, 335903614\n"
    "v_mov_b32 v6, s66\n"
    "v_mov_b32 v7, s67\n"
    "v_mov_b32 v5, 163776\n"
    "ds_write_b64 v5, v[6:7]\n"
    "s_lshr_b32 s60, s3, 1\n"
    "s_mov_b32 s63, 0\n"
    "s_waitcnt lgkmcnt(0)\n"
    ".Lk_seg:\n"
    "s_barrier\n"
    "s_cmp_eq_u32 s63, s60\n"
    "s_cbranch_scc1 .Lk_tr\n"
    "ds_read_b128 v[24:27], v40\n"
    "ds_read_b128 v[28:31], v40 offset:1024\n"
    "ds_read_b128 v[32:35], v40 offset:2048\n"
    "ds_read_b32 v36, v42 offset:3072\n"
    "s_waitcnt lgkmcnt(3)\n"
    "v_add_f32 v8, v8, v24\n"
    "v_add_f32 v9, v9, v25\n"
    "v_add_f32 v10, v10, v26\n"
    "v_add_f32 v11, v11, v27\n"
    "s_waitcnt lgkmcnt(2)\n"
    "v_add_f32 v12, v12, v28\n"
    "v_add_f32 v13, v13, v29\n"
    "v_add_f32 v14, v14, v30\n"
    "v_add_f32 v15, v15, v31\n"
    "s_waitcnt lgkmcnt(1)\n"
    "v_add_f32 v16, v16, v32\n"
    "v_add_f32 v17, v17, v33\n"
    "v_add_f32 v18, v18, v34\n"
    "v_add_f32 v19, v19, v35\n"
    "s_waitcnt lgkmcnt(0)\n"
    "v_add_f32 v20, v20, v36\n"
    "s_branch .Lk_segx\n"
    ".Lk_tr:\n"
    "s_and_b32 s50, s3, 1\n"
    "s_lshl1_add_u32 s50, s50, s41\n"
    "s_add_u32 s51, s48, s60\n"
    "s_and_b32 s51, s51, 7\n"
    "s_lshl2_add_u32 s61, s51, s50\n"
    "s_mul_i32 s51, s60, 40992\n"
    "s_lshl_b32 s50, s50, 5\n"
    "v_bfe_u32 v44, v1, 4, 1\n"
    "v_and_b32 v45, 3, v1\n"
    "v_bfe_u32 v46, v1, 2, 2\n"
    "v_lshrrev_b32 v47, 5, v1\n"
    "v_lshl_add_u32 v48, v44, 2, v45\n"
    "v_lshl_add_u32 v48, v48, 4, v46\n"
    "v_lshl_add_u32 v48, v48, 7, s51\n"
    "v_lshl_add_u32 v48, v47, 3, v48\n"
    "v_and_b32 v49, 8, v1\n"
    "v_lshl_or_b32 v49, v49, 1, s50\n"
    "v_xad_u32 v50, v49, 0, v48\n"
    "ds_read_b64_tr_b16 v[24:25], v50\n"
    "v_xad_u32 v51, v49, 32, v48\n"
    "ds_read_b64_tr_b16 v[26:27], v51 offset:512\n"
    "v_xad_u32 v52, v49, 64, v48\n"
    "ds_read_b64_tr_b16 v[28:29], v52 offset:1024\n"
    "s_movk_i32 s52, 96\n"
    "v_xad_u32 v53, v49, s52, v48\n"
    "ds_read_b64_tr_b16 v[30:31], v53 offset:1536\n"
    "v_xad_u32 v54, v49, 16, v48\n"
    "ds_read_b64_tr_b16 v[32:33], v54\n"
    "v_xad_u32 v55, v49, 48, v48\n"
    "ds_read_b64_tr_b16 v[34:35], v55 offset:512\n"
    "s_movk_i32 s52, 80\n"
    "v_xad_u32 v56, v49, s52, v48\n"
    "ds_read_b64_tr_b16 v[36:37], v56 offset:1024\n"
    "s_movk_i32 s52, 112\n"
    "v_xad_u32 v57, v49, s52, v48\n"
    "ds_read_b64_tr_b16 v[38:39], v57 offset:1536\n"
    "v_lshl_add_u32 v48, v44, 2, v46\n"
    "v_lshrrev_b32 v49, 1, v45\n"
    "v_lshl_add_u32 v48, v48, 1, v49\n"
    "v_xor_b32 v49, v45, v44\n"
    "v_and_b32 v49, 1, v49\n"
    "v_lshl_add_u32 v48, v48, 1, v49\n"
    "v_lshlrev_b32 v48, 4, v48\n"
    "v_lshl_add_u32 v48, v47, 14, v48\n"
    "v_add_u32 v49, 8192, v48\n"
    "s_lshl_b32 s50, s61, 16\n"
    "s_lshl_b32 s51, s44, 9\n"
    "s_add_u32 s50, s50, s51\n"
    "s_add_u32 s50, s50, 524288\n"
    "s_mov_b32 vcc_lo, 858993459\n"
    "s_mov_b32 vcc_hi, 858993459\n"
    "s_waitcnt lgkmcnt(5)\n"
    "v_cndmask_b32_dpp v52, v28, v24, vcc quad_perm:[2,3,0,1] row_mask:0xf bank_mask:0xf\n"
    "v_cndmask_b32_dpp v53, v29, v25, vcc quad_perm:[2,3,0,1] row_mask:0xf bank_mask:0xf\n"
    "s_waitcnt lgkmcnt(4)\n"
    "v_cndmask_b32_dpp v54, v30, v26, vcc quad_perm:[2,3,0,1] row_mask:0xf bank_mask:0xf\n"
    "v_cndmask_b32_dpp v55, v31, v27, vcc quad_perm:[2,3,0,1] row_mask:0xf bank_mask:0xf\n"
    "s_not_b64 vcc, vcc\n"
    "buffer_store_dwordx4 v[52:55], v48, s[12:15], s50 offen sc1\n"
    "v_cndmask_b32_dpp v56, v24, v28, vcc quad_perm:[2,3,0,1] row_mask:0xf bank_mask:0xf\n"
    "v_cndmask_b32_dpp v57, v25, v29, vcc quad_perm:[2,3,0,1] row_mask:0xf bank_mask:0xf\n"
    "v_cndmask_b32_dpp v58, v26, v30, vcc quad_perm:[2,3,0,1] row_mask:0xf bank_mask:0xf\n"
    "v_cndmask_b32_dpp v59, v27, v31, vcc quad_perm:[2,3,0,1] row_mask:0xf bank_mask:0xf\n"
    "buffer_store_dwordx4 v[56:59], v49, s[12:15], s50 offen sc1\n"
    "s_add_u32 s50, s50, 32768\n"
    "s_mov_b32 vcc_lo, 858993459\n"
    "s_mov_b32 vcc_hi, 858993459\n"
    "s_waitcnt lgkmcnt(1)\n"
    "v_cndmask_b32_dpp v52, v36, v32, vcc quad_perm:[2,3,0,1] row_mask:0xf bank_mask:0xf\n"
    "v_cndmask_b32_dpp v53, v37, v33, vcc quad_perm:[2,3,0,1] row_mask:0xf bank_mask:0xf\n"
    "s_waitcnt lgkmcnt(0)\n"
    "v_cndmask_b32_dpp v54, v38, v34, vcc quad_perm:[2,3,0,1] row_mask:0xf bank_mask:0xf\n"
    "v_cndmask_b32_dpp v55, v39, v35, vcc quad_perm:[2,3,0,1] row_mask:0xf bank_mask:0xf\n"
    "s_not_b64 vcc, vcc\n"
    "buffer_store_dwordx4 v[52:55], v48, s[12:15], s50 offen sc1\n"
    "v_cndmask_b32_dpp v56, v32, v36, vcc quad_perm:[2,3,0,1] row_mask:0xf bank_mask:0xf\n"
    "v_cndmask_b32_dpp v57, v33, v37, vcc quad_perm:[2,3,0,1] row_mask:0xf bank_mask:0xf\n"
    "v_cndmask_b32_dpp v58, v34, v38, vcc quad_perm:[2,3,0,1] row_mask:0xf bank_mask:0xf\n"
    "v_cndmask_b32_dpp v59, v35, v39, vcc quad_perm:[2,3,0,1] row_mask:0xf bank_mask:0xf\n"
    "buffer_store_dwordx4 v[56:59], v49, s[12:15], s50 offen sc1\n"
    ".Lk_segx:\n"
    "s_add_u32 s63, s63, 1\n"
    "s_cmp_lt_u32 s63, 2\n"
    "s_cbranch_scc1 .Lk_seg\n"
    "s_barrier\n"
    "s_lshl_b32 s50, s41, 4\n"
    "s_xor_b32 s50, s50, 16\n"
    "s_lshl_b32 s50, s59, s50\n"
    "s_and_b32 s50, s50, 4294901760\n"
    "v_mov_b32 v24, 0x1f800000\n"
    "s_mov_b64 exec, 1\n"
    "v_fmac_f32 v8, s50, v24\n"
    "s_mov_b64 exec, -1\n"
    "v_cvt_pk_bf16_f32 v73, v14, v15\n"
    "v_cvt_pk_bf16_f32 v75, v13, v14\n"
    "v_mov_b32 v68, 0\n"
    "v_mov_b32 v70, 0\n"
    "v_cvt_pk_bf16_f32 v64, v8, v9\n"
    "v_cvt_pk_bf16_f32 v69, v8, v9\n"
    "v_cvt_pk_bf16_f32 v65, v10, v11\n"
    "v_cvt_pk_bf16_f32 v76, v10, v11\n"
    "v_cvt_pk_bf16_f32 v72, v12, v13\n"
    "v_cvt_pk_bf16_f32 v77, v12, v13\n"
    "v_cvt_pk_bf16_f32 v67, v9, v10\n"
    "v_cvt_pk_bf16_f32 v78, v9, v10\n"
    "v_cvt_pk_bf16_f32 v74, v11, v12\n"
    "v_cvt_pk_bf16_f32 v79, v11, v12\n"
    "v_mov_b32_dpp v68, v73 wave_shr:1 row_mask:0xf bank_mask:0xf\n"
    "v_mov_b32_dpp v70, v75 wave_shr:1 row_mask:0xf bank_mask:0xf\n"
    "v_readlane_b32 s52, v73, 63\n"
    "v_readlane_b32 s53, v75, 63\n"
    "v_alignbit_b32 v66, v64, v68, 16\n"
    "v_alignbit_b32 v71, v64, v68, 16\n"
    "v_cvt_pk_bf16_f32 v89, v18, v18\n"
    "v_cvt_pk_bf16_f32 v91, v17, v18\n"
    "v_mov_b32 v84, s52\n"
    "v_mov_b32 v86, s53\n"
    "v_cvt_pk_bf16_f32 v80, v16, v16\n"
    "v_cvt_pk_bf16_f32 v85, v16, v16\n"
    "v_cvt_pk_bf16_f32 v81, v16, v17\n"
    "v_cvt_pk_bf16_f32 v92, v16, v17\n"
    "v_cvt_pk_bf16_f32 v88, v17, v17\n"
    "v_cvt_pk_bf16_f32 v93, v17, v17\n"
    "v_cvt_pk_bf16_f32 v83, v16, v16\n"
    "v_cvt_pk_bf16_f32 v94, v16, v16\n"
    "v_cvt_pk_bf16_f32 v90, v17, v17\n"
    "v_cvt_pk_bf16_f32 v95, v17, v17\n"
    "v_mov_b32_dpp v84, v89 wave_shr:1 row_mask:0xf bank_mask:0xf\n"
    "v_mov_b32_dpp v86, v91 wave_shr:1 row_mask:0xf bank_mask:0xf\n"
    "v_readlane_b32 s52, v89, 63\n"
    "v_readlane_b32 s53, v91, 63\n"
    "v_alignbit_b32 v82, v80, v84, 16\n"
    "v_alignbit_b32 v87, v80, v84, 16\n"
    "s_lshl4_add_u32 s50, s61, s44\n"
    "s_lshl_b32 s50, s50, 7\n"
    "s_add_u32 s50, s50, 16384\n"
    "s_waitcnt vmcnt(0)\n"
    "s_mov_b64 exec, 1\n"
    "buffer_store_dwordx2 v[6:7], off, s[12:15], s50 sc1\n"
    "s_mov_b64 exec, -1\n"
    "s_barrier\n"
    "v_mov_b32 v96, 0\n"
    "v_mov_b32 v97, 0\n"
    "v_mov_b32 v98, 0\n"
    "v_mov_b32 v99, 0\n"
    "v_mov_b32 v100, 0\n"
    "v_mov_b32 v101, 0\n"
    "s_and_b32 s54, s52, 65535\n"
    "s_mov_b64 exec, 15\n"
    "v_mov_b32 v96, s52\n"
    "v_mov_b32 v97, s52\n"
    "v_mov_b32 v98, s52\n"
    "v_mov_b32 v99, s52\n"
    "s_mov_b64 exec, -1\n"
    "v_writelane_b32 v98, s53, 0\n"
    "v_writelane_b32 v97, s54, 2\n"
    "v_writelane_b32 v96, s54, 3\n"
    "v_writelane_b32 v97, 0, 3\n"
    "v_writelane_b32 v99, 0, 3\n"
    "v_cvt_pk_bf16_f32 v24, v19, v20\n"
    "s_mov_b32 s50, 84148480\n"
    "s_mov_b32 s51, 117834498\n"
    "v_mov_b32_dpp v25, v24 wave_ror:1 row_mask:0xf bank_mask:0xf\n"
    "v_lshrrev_b32 v27, 1, v1\n"
    "v_or_b32 v28, 1, v27\n"
    "v_mov_b32_dpp v26, v25 wave_ror:1 row_mask:0xf bank_mask:0xf\n"
    "s_mov_b64 exec, 3\n"
    "v_lshlrev_b32 v26, 16, v26\n"
    "s_mov_b64 exec, -1\n"
    "v_perm_b32 v102, v24, v26, s50\n"
    "v_perm_b32 v103, v24, v26, s51\n"
    "s_mul_i32 s50, s41, 9920\n"
    "s_add_u32 s50, s50, 144096\n"
    "v_and_b32 v24, 1, v1\n"
    "v_mul_u32_u24 v24, 672, v24\n"
    "v_sub_u32 v24, s50, v24\n"
    "v_sub_u32 v27, v28, v27\n"
    "v_lshl_add_u32 v24, v27, 3, v24\n"
    "v_lshrrev_b32 v25, 2, v28\n"
    "v_lshl_add_u32 v25, v25, 5, v24\n"
    "v_and_b32 v26, 2, v28\n"
    "v_lshl_add_u32 v104, v26, 1, v25\n"
    "v_add_u32 v28, 2, v28\n"
    "v_add_u32 v24, 16, v24\n"
    "v_lshrrev_b32 v25, 2, v28\n"
    "v_lshl_add_u32 v25, v25, 5, v24\n"
    "v_and_b32 v26, 2, v28\n"
    "v_lshl_add_u32 v105, v26, 1, v25\n"
    "s_barrier\n"
    "s_branch .Lk_cvset\n"
    ".Lk_cvret:\n"
    "v_and_b32 v31, 15, v1\n"
    "v_lshlrev_b32 v31, 7, v31\n"
    "s_lshl_b32 s64, s43, 11\n"
    "s_add_u32 s64, s64, 16384\n"
    "s_barrier\n"
    "s_mul_i32 s50, s41, 9920\n"
    "s_add_u32 s50, s50, 134720\n"
    "s_add_u32 s51, s50, 8320\n"
    "v_lshl_add_u32 v30, v1, 4, s51\n"
    "v_and_b32 v25, 7, v1\n"
    "s_mov_b32 exec_lo, 0\n"
    "v_add_u32 v30, 416, v30\n"
    "s_mov_b32 exec_hi, 4294901760\n"
    "v_lshl_add_u32 v30, v25, 4, s50\n"
    "s_mov_b64 exec, -1\n"
    "s_add_u32 s50, s50, 128\n"
    "v_lshl_add_u32 v24, v1, 6, s50\n"
    "v_add_u32 v25, 2, v1\n"
    "v_and_b32 v25, 6, v25\n"
    "v_lshlrev_b32 v25, 3, v25\n"
    "v_xad_u32 v26, v25, 0, v24\n"
    "v_xad_u32 v27, v25, 16, v24\n"
    "v_xad_u32 v28, v25, 32, v24\n"
    "v_xad_u32 v29, v25, 48, v24\n"
    "v_lshlrev_b32 v34, 4, v1\n"
    "s_lshl1_add_u32 s50, s2, s41\n"
    "s_lshl_b32 s50, s50, 12\n"
    "s_add_u32 s65, s50, 524288\n"
    "s_lshl_b32 s50, s41, 12\n"
    "s_add_u32 s50, s50, 155584\n"
    "v_add_u32 v35, s50, v34\n"
    "s_lshl_b32 s50, s41, 9\n"
    "s_add_u32 s50, s50, 154560\n"
    "v_lshl_add_u32 v40, v1, 3, s50\n"
    "s_lshl_b32 s50, s41, 2\n"
    "s_add_u32 s50, s50, 163800\n"
    "v_mov_b32 v41, s50\n"
    "s_barrier\n"
    "s_cmp_eq_u32 s45, 15\n"
    "s_cbranch_scc0 .Lk_up\n"
    "s_barrier\n"
    "s_sleep 30\n"
    "s_branch .Lk_upl\n"
    ".Lk_up:\n"
    "buffer_load_dwordx2 v[32:33], v31, s[12:15], s64 offen sc1\n"
    "s_barrier\n"
    "s_waitcnt vmcnt(0)\n"
    "v_cmp_ne_u64 vcc, s[66:67], v[32:33]\n"
    "s_and_b64 vcc, exec, vcc\n"
    "s_cbranch_vccz .Lk_uok\n"
    ".Lk_upl:\n"
    "s_mov_b32 s69, 65536\n"
    ".Lk_l20:\n"
    "buffer_load_dwordx2 v[32:33], v31, s[12:15], s64 offen sc1\n"
    "s_waitcnt vmcnt(0)\n"
    "v_cmp_ne_u64 vcc, s[66:67], v[32:33]\n"
    "s_and_b64 vcc, exec, vcc\n"
    "s_cbranch_vccz .Lk_l21\n"
    "s_sleep 2\n"
    "s_sub_u32 s69, s69, 1\n"
    "s_cbranch_scc0 .Lk_l20\n"
    ".Lk_l21:\n"
    ".Lk_uok:\n"
    "buffer_load_dwordx4 v[128:131], v34, s[12:15], s65 offen sc1\n"
    "buffer_load_dwordx4 v[132:135], v34, s[12:15], s65 offen offset:1024 sc1\n"
    "buffer_load_dwordx4 v[136:139], v34, s[12:15], s65 offen offset:2048 sc1\n"
    "buffer_load_dwordx4 v[140:143], v34, s[12:15], s65 offen offset:3072 sc1\n"
    "s_setprio 3\n"
    "s_mov_b32 s69, 4194304\n"
    ".Lk_l22:\n"
    "ds_read_b32 v161, v164\n"
    "s_waitcnt lgkmcnt(0)\n"
    "v_cmp_gt_u32 vcc, 8, v161\n"
    "s_and_b64 vcc, exec, vcc\n"
    "s_cbranch_vccz .Lk_l23\n"
    "s_sleep 1\n"
    "s_sub_u32 s69, s69, 1\n"
    "s_cbranch_scc0 .Lk_l22\n"
    ".Lk_l23:\n"
    "ds_write_b128 v30, v[96:99]\n"
    "ds_write_b64 v40, v[100:101]\n"
    "ds_write2_b32 v104, v102, v103 offset1:64\n"
    "ds_write2_b32 v105, v102, v103 offset1:64\n"
    "ds_write_b128 v26, v[64:67] offset:0\n"
    "ds_write_b128 v27, v[68:71] offset:0\n"
    "ds_write_b128 v28, v[72:75] offset:0\n"
    "ds_write_b128 v29, v[76:79] offset:0\n"
    "ds_write_b128 v26, v[80:83] offset:4096\n"
    "ds_write_b128 v27, v[84:87] offset:4096\n"
    "ds_write_b128 v28, v[88:91] offset:4096\n"
    "ds_write_b128 v29, v[92:95] offset:4096\n"
    "s_waitcnt vmcnt(3)\n"
    "ds_write_b128 v35, v[128:131] offset:0\n"
    "s_waitcnt vmcnt(2)\n"
    "ds_write_b128 v35, v[132:135] offset:1024\n"
    "s_waitcnt vmcnt(1)\n"
    "ds_write_b128 v35, v[136:139] offset:2048\n"
    "s_waitcnt vmcnt(0)\n"
    "ds_write_b128 v35, v[140:143] offset:3072\n"
    "v_mov_b32 v42, 2\n"
    "ds_write_b32 v41, v42\n"
    ".Lk_conv:\n"
    "s_setprio 0\n"
    "s_mov_b32 s69, 4194304\n"
    ".Lk_l24:\n"
    "ds_read_b32 v161, v163\n"
    "s_waitcnt lgkmcnt(0)\n"
    "v_cmp_gt_u32 vcc, 2, v161\n"
    "s_and_b64 vcc, exec, vcc\n"
    "s_cbranch_vccz .Lk_l25\n"
    "s_sleep 1\n"
    "s_sub_u32 s69, s69, 1\n"
    "s_cbranch_scc0 .Lk_l24\n"
    ".Lk_l25:\n"
    "ds_read_b64 v[166:167], v160\n"
    "ds_read_b64_tr_b16 v[8:9], v152 offset:1536\n"
    "ds_read_b64_tr_b16 v[10:11], v153 offset:1536\n"
    "ds_read_b128 v[24:27], v155\n"
    "ds_read_b128 v[28:31], v155 offset:1024\n"
    "ds_read_b128 v[32:35], v155 offset:2048\n"
    "ds_read_b128 v[36:39], v155 offset:3072\n"
    "ds_read_b128 v[40:43], v155 offset:4096\n"
    "ds_read_b128 v[44:47], v155 offset:5120\n"
    "ds_read_b128 v[48:51], v155 offset:6144\n"
    "ds_read_b128 v[52:55], v155 offset:7168\n"
    "ds_read_b64_tr_b16 v[12:13], v152 offset:3584\n"
    "ds_read_b64_tr_b16 v[14:15], v153 offset:3584\n"
    "ds_read_b64_tr_b16 v[16:17], v152 offset:5632\n"
    "ds_read_b64_tr_b16 v[18:19], v153 offset:5632\n"
    "ds_read_b64_tr_b16 v[20:21], v152 offset:7680\n"
    "ds_read_b64_tr_b16 v[22:23], v153 offset:7680\n"
    "v_lshlrev_b32 v161, 3, v1\n"
    "s_waitcnt lgkmcnt(14)\n"
    "v_lshrrev_b32 v161, v161, v166\n"
    "v_and_b32 v161, 252, v161\n"
    "v_and_b32 v162, 1, v166\n"
    "v_add3_u32 v161, v161, v162, 1\n"
    "v_lshl_or_b32 v161, v161, 15, v161\n"
    "s_mov_b32 s55, 1073758208\n"
    "v_mov_b32 v180, v161\n"
    "v_mov_b32 v162, 2\n"
    "v_subrev_u32 v160, 16, v165\n"
    "s_mov_b32 s69, 4194304\n"
    ".Lk_l26:\n"
    "ds_read_b32 v161, v164\n"
    "s_waitcnt lgkmcnt(0)\n"
    "v_cmp_gt_u32 vcc, 9, v161\n"
    "s_and_b64 vcc, exec, vcc\n"
    "s_cbranch_vccz .Lk_l27\n"
    "s_sleep 1\n"
    "s_sub_u32 s69, s69, 1\n"
    "s_cbranch_scc0 .Lk_l26\n"
    ".Lk_l27:\n"
    ".p2align 3 \n"
    "v_mfma_f32_16x16x32_bf16 v[192:195], v[8:11], v[24:27], 0\n"
    "ds_read_b64_tr_b16 v[56:57], v152 offset:1024\n"
    "v_mfma_f32_16x16x32_bf16 v[196:199], v[8:11], v[28:31], 0\n"
    "ds_read_b64_tr_b16 v[58:59], v153 offset:1024\n"
    "v_mfma_f32_16x16x32_bf16 v[200:203], v[8:11], v[32:35], 0\n"
    "ds_read_b128 v[72:75], v154 offset:256\n"
    "v_mfma_f32_16x16x32_bf16 v[204:207], v[8:11], v[36:39], 0\n"
    "ds_read_b128 v[76:79], v154 offset:1280\n"
    "v_mfma_f32_16x16x32_bf16 v[208:211], v[8:11], v[40:43], 0\n"
    "ds_read_b128 v[80:83], v154 offset:2304\n"
    "v_mfma_f32_16x16x32_bf16 v[212:215], v[8:11], v[44:47], 0\n"
    "ds_read_b128 v[84:87], v154 offset:3328\n"
    "v_mfma_f32_16x16x32_bf16 v[216:219], v[8:11], v[48:51], 0\n"
    "ds_read_b128 v[88:91], v154 offset:4352\n"
    "v_mfma_f32_16x16x32_bf16 v[220:223], v[8:11], v[52:55], 0\n"
    "ds_read_b128 v[92:95], v154 offset:5376\n"
    "v_mfma_f32_16x16x32_bf16 v[196:199], v[12:15], v[24:27], v[196:199]\n"
    "ds_read_b128 v[96:99], v154 offset:6400\n"
    "v_mfma_f32_16x16x32_bf16 v[200:203], v[12:15], v[28:31], v[200:203]\n"
    "ds_read_b128 v[100:103], v154 offset:7424\n"
    "v_mfma_f32_16x16x32_bf16 v[204:207], v[12:15], v[32:35], v[204:207]\n"
    "ds_read_b64_tr_b16 v[60:61], v152 offset:3072\n"
    "v_mfma_f32_16x16x32_bf16 v[208:211], v[12:15], v[36:39], v[208:211]\n"
    "ds_read_b64_tr_b16 v[62:63], v153 offset:3072\n"
    "v_mfma_f32_16x16x32_bf16 v[212:215], v[12:15], v[40:43], v[212:215]\n"
    "ds_read_b64_tr_b16 v[64:65], v152 offset:5120\n"
    "v_mfma_f32_16x16x32_bf16 v[216:219], v[12:15], v[44:47], v[216:219]\n"
    "ds_read_b64_tr_b16 v[66:67], v153 offset:5120\n"
    "v_mfma_f32_16x16x32_bf16 v[220:223], v[12:15], v[48:51], v[220:223]\n"
    "ds_read_b64_tr_b16 v[68:69], v152 offset:7168\n"
    "v_mfma_f32_16x16x32_bf16 v[200:203], v[16:19], v[24:27], v[200:203]\n"
    "ds_read_b64_tr_b16 v[70:71], v153 offset:7168\n"
    "v_mfma_f32_16x16x32_bf16 v[204:207], v[16:19], v[28:31], v[204:207]\n"
    "ds_read_b128 v[104:107], v232\n"
    "v_mfma_f32_16x16x32_bf16 v[208:211], v[16:19], v[32:35], v[208:211]\n"
    "ds_read_b128 v[108:111], v233\n"
    "v_mfma_f32_16x16x32_bf16 v[212:215], v[16:19], v[36:39], v[212:215]\n"
    "ds_read_b128 v[112:115], v232 offset:1024\n"
    "v_mfma_f32_16x16x32_bf16 v[216:219], v[16:19], v[40:43], v[216:219]\n"
    "ds_read_b128 v[116:119], v233 offset:1024\n"
    "v_mfma_f32_16x16x32_bf16 v[220:223], v[16:19], v[44:47], v[220:223]\n"
    "ds_read_b64_tr_b16 v[120:121], v234\n"
    "v_mfma_f32_16x16x32_bf16 v[204:207], v[20:23], v[24:27], v[204:207]\n"
    "ds_read_b64_tr_b16 v[122:123], v234 offset:672\n"
    "v_mfma_f32_16x16x32_bf16 v[208:211], v[20:23], v[28:31], v[208:211]\n"
    "ds_read_b64_tr_b16 v[124:125], v234 offset:128\n"
    "v_mfma_f32_16x16x32_bf16 v[212:215], v[20:23], v[32:35], v[212:215]\n"
    "ds_read_b64_tr_b16 v[126:127], v234 offset:800\n"
    "v_mfma_f32_16x16x32_bf16 v[216:219], v[20:23], v[36:39], v[216:219]\n"
    "ds_read_b64_tr_b16 v[128:129], v235\n"
    "v_mfma_f32_16x16x32_bf16 v[220:223], v[20:23], v[40:43], v[220:223]\n"
    "ds_read_b64_tr_b16 v[130:131], v236\n"
    "s_waitcnt lgkmcnt(14)\n"
    "v_mfma_f32_16x16x32_bf16 v[192:195], v[56:59], v[72:75], v[192:195]\n"
    "ds_read_b64_tr_b16 v[8:9], v152 offset:512\n"
    "v_mfma_f32_16x16x32_bf16 v[196:199], v[56:59], v[76:79], v[196:199]\n"
    "ds_read_b64_tr_b16 v[10:11], v153 offset:512\n"
    "v_mfma_f32_16x16x32_bf16 v[200:203], v[56:59], v[80:83], v[200:203]\n"
    "ds_read_b128 v[24:27], v155 offset:512\n"
    "v_mfma_f32_16x16x32_bf16 v[204:207], v[56:59], v[84:87], v[204:207]\n"
    "ds_read_b128 v[28:31], v155 offset:1536\n"
    "s_waitcnt lgkmcnt(13)\n"
    "v_mfma_f32_16x16x32_bf16 v[136:139], v[104:107], v[224:227], 0\n"
    "ds_read_b128 v[32:35], v155 offset:2560\n"
    "s_waitcnt lgkmcnt(13)\n"
    "v_mfma_f32_16x16x32_bf16 v[140:143], v[108:111], v[228:231], 0\n"
    "ds_read_b128 v[36:39], v155 offset:3584\n"
    "s_waitcnt lgkmcnt(13)\n"
    "v_mfma_f32_16x16x32_bf16 v[144:147], v[112:115], v[224:227], 0\n"
    "ds_read_b128 v[40:43], v155 offset:4608\n"
    "s_waitcnt lgkmcnt(13)\n"
    "v_mfma_f32_16x16x32_bf16 v[148:151], v[116:119], v[228:231], 0\n"
    "ds_read_b128 v[44:47], v155 offset:5632\n"
    "v_mfma_f32_16x16x32_bf16 v[208:211], v[56:59], v[88:91], v[208:211]\n"
    "ds_read_b128 v[48:51], v155 offset:6656\n"
    "v_mfma_f32_16x16x32_bf16 v[212:215], v[56:59], v[92:95], v[212:215]\n"
    "ds_read_b128 v[52:55], v155 offset:7680\n"
    "v_mfma_f32_16x16x32_bf16 v[216:219], v[56:59], v[96:99], v[216:219]\n"
    "ds_read_b64_tr_b16 v[12:13], v152 offset:2560\n"
    "v_mfma_f32_16x16x32_bf16 v[220:223], v[56:59], v[100:103], v[220:223]\n"
    "ds_read_b64_tr_b16 v[14:15], v153 offset:2560\n"
    "v_mfma_f32_16x16x32_bf16 v[196:199], v[60:63], v[72:75], v[196:199]\n"
    "ds_read_b64_tr_b16 v[16:17], v152 offset:4608\n"
    "v_mfma_f32_16x16x32_bf16 v[200:203], v[60:63], v[76:79], v[200:203]\n"
    "ds_read_b64_tr_b16 v[18:19], v153 offset:4608\n"
    "v_mfma_f32_16x16x32_bf16 v[204:207], v[60:63], v[80:83], v[204:207]\n"
    "ds_read_b64_tr_b16 v[20:21], v152 offset:6656\n"
    "v_mfma_f32_16x16x32_bf16 v[208:211], v[60:63], v[84:87], v[208:211]\n"
    "ds_read_b64_tr_b16 v[22:23], v153 offset:6656\n"
    "v_mfma_f32_16x16x32_bf16 v[212:215], v[60:63], v[88:91], v[212:215]\n"
    "ds_read_b64_tr_b16 v[132:133], v234 offset:384\n"
    "v_mfma_f32_16x16x32_bf16 v[216:219], v[60:63], v[92:95], v[216:219]\n"
    "ds_read_b64_tr_b16 v[134:135], v234 offset:1056\n"
    "v_mfma_f32_16x16x32_bf16 v[220:223], v[60:63], v[96:99], v[220:223]\n"
    "v_cvt_pk_bf16_f32 v104, v136, v137\n"
    "v_mfma_f32_16x16x32_bf16 v[200:203], v[64:67], v[72:75], v[200:203]\n"
    "v_cvt_pk_bf16_f32 v105, v138, v139\n"
    "v_mfma_f32_16x16x32_bf16 v[204:207], v[64:67], v[76:79], v[204:207]\n"
    "v_cvt_pk_bf16_f32 v106, v140, v141\n"
    "v_mfma_f32_16x16x32_bf16 v[208:211], v[64:67], v[80:83], v[208:211]\n"
    "v_cvt_pk_bf16_f32 v107, v142, v143\n"
    "v_mfma_f32_16x16x32_bf16 v[212:215], v[64:67], v[84:87], v[212:215]\n"
    "v_cvt_pk_bf16_f32 v108, v144, v145\n"
    "v_mfma_f32_16x16x32_bf16 v[216:219], v[64:67], v[88:91], v[216:219]\n"
    "v_cvt_pk_bf16_f32 v109, v146, v147\n"
    "v_mfma_f32_16x16x32_bf16 v[220:223], v[64:67], v[92:95], v[220:223]\n"
    "v_cvt_pk_bf16_f32 v110, v148, v149\n"
    "v_mfma_f32_16x16x32_bf16 v[204:207], v[68:71], v[72:75], v[204:207]\n"
    "v_cvt_pk_bf16_f32 v111, v150, v151\n"
    "v_mfma_f32_16x16x32_bf16 v[208:211], v[68:71], v[76:79], v[208:211]\n"
    "v_mfma_f32_16x16x32_bf16 v[212:215], v[68:71], v[80:83], v[212:215]\n"
    "v_mfma_f32_16x16x32_bf16 v[216:219], v[68:71], v[84:87], v[216:219]\n"
    "v_mfma_f32_16x16x32_bf16 v[220:223], v[68:71], v[88:91], v[220:223]\n"
    "s_waitcnt lgkmcnt(14)\n"
    "v_mfma_f32_16x16x32_bf16 v[192:195], v[8:11], v[24:27], v[192:195]\n"
    "ds_read_b64_tr_b16 v[56:57], v152\n"
    "v_mfma_f32_16x16x32_bf16 v[196:199], v[8:11], v[28:31], v[196:199]\n"
    "ds_read_b64_tr_b16 v[58:59], v153\n"
    "s_waitcnt lgkmcnt(14)\n"
    "v_mfma_f32_16x16x32_bf16 v[200:203], v[8:11], v[32:35], v[200:203]\n"
    "ds_read_b128 v[72:75], v154 offset:768\n"
    "v_mfma_f32_16x16x32_bf16 v[204:207], v[8:11], v[36:39], v[204:207]\n"
    "ds_read_b128 v[76:79], v154 offset:1792\n"
    "s_waitcnt lgkmcnt(14)\n"
    "v_mfma_f32_16x16x32_bf16 v[208:211], v[8:11], v[40:43], v[208:211]\n"
    "ds_read_b128 v[80:83], v154 offset:2816\n"
    "v_mfma_f32_16x16x32_bf16 v[212:215], v[8:11], v[44:47], v[212:215]\n"
    "ds_read_b128 v[84:87], v154 offset:3840\n"
    "s_waitcnt lgkmcnt(14)\n"
    "v_mfma_f32_16x16x32_bf16 v[216:219], v[8:11], v[48:51], v[216:219]\n"
    "ds_read_b128 v[88:91], v154 offset:4864\n"
    "v_mfma_f32_16x16x32_bf16 v[220:223], v[8:11], v[52:55], v[220:223]\n"
    "ds_read_b128 v[92:95], v154 offset:5888\n"
    "s_waitcnt lgkmcnt(14)\n"
    "v_mfma_f32_16x16x32_bf16 v[196:199], v[12:15], v[24:27], v[196:199]\n"
    "ds_read_b128 v[96:99], v154 offset:6912\n"
    "v_mfma_f32_16x16x32_bf16 v[200:203], v[12:15], v[28:31], v[200:203]\n"
    "ds_read_b128 v[100:103], v154 offset:7936\n"
    "v_mfma_f32_16x16x32_bf16 v[204:207], v[12:15], v[32:35], v[204:207]\n"
    "ds_read_b64_tr_b16 v[60:61], v152 offset:2048\n"
    "v_mfma_f32_16x16x32_bf16 v[208:211], v[12:15], v[36:39], v[208:211]\n"
    "ds_read_b64_tr_b16 v[62:63], v153 offset:2048\n"
    "v_mfma_f32_16x16x32_bf16 v[212:215], v[12:15], v[40:43], v[212:215]\n"
    "ds_read_b64_tr_b16 v[64:65], v152 offset:4096\n"
    "v_mfma_f32_16x16x32_bf16 v[216:219], v[12:15], v[44:47], v[216:219]\n"
    "ds_read_b64_tr_b16 v[66:67], v153 offset:4096\n"
    "v_mfma_f32_16x16x32_bf16 v[220:223], v[12:15], v[48:51], v[220:223]\n"
    "ds_read_b64_tr_b16 v[68:69], v152 offset:6144\n"
    "s_waitcnt lgkmcnt(14)\n"
    "v_mfma_f32_16x16x32_bf16 v[200:203], v[16:19], v[24:27], v[200:203]\n"
    "ds_read_b64_tr_b16 v[70:71], v153 offset:6144\n"
    "v_mfma_f32_16x16x32_bf16 v[204:207], v[16:19], v[28:31], v[204:207]\n"
    "ds_write_b32 v165, v162\n"
    "v_mfma_f32_16x16x32_bf16 v[208:211], v[16:19], v[32:35], v[208:211]\n"
    "v_lshlrev_b32 v176, 14, v180\n"
    "v_mfma_f32_16x16x32_bf16 v[212:215], v[16:19], v[36:39], v[212:215]\n"
    "v_and_b32 v176, s55, v176\n"
    "v_mfma_f32_16x16x32_bf16 v[216:219], v[16:19], v[40:43], v[216:219]\n"
    "v_lshlrev_b32 v177, 12, v180\n"
    "v_mfma_f32_16x16x32_bf16 v[220:223], v[16:19], v[44:47], v[220:223]\n"
    "v_and_b32 v177, s55, v177\n"
    "v_mfma_f32_16x16x32_bf16 v[204:207], v[20:23], v[24:27], v[204:207]\n"
    "v_lshlrev_b32 v178, 10, v180\n"
    "v_mfma_f32_16x16x32_bf16 v[208:211], v[20:23], v[28:31], v[208:211]\n"
    "v_and_b32 v178, s55, v178\n"
    "v_mfma_f32_16x16x32_bf16 v[212:215], v[20:23], v[32:35], v[212:215]\n"
    "v_lshlrev_b32 v179, 8, v180\n"
    "v_mfma_f32_16x16x32_bf16 v[216:219], v[20:23], v[36:39], v[216:219]\n"
    "v_and_b32 v179, s55, v179\n"
    "v_mfma_f32_16x16x32_bf16 v[220:223], v[20:23], v[40:43], v[220:223]\n"
    "s_cmp_ge_u32 s40, 6\n"
    "s_cbranch_scc1 .Lk_cv1\n"
    "s_waitcnt lgkmcnt(13)\n"
    "v_mfma_f32_16x16x32_bf16 v[196:199], v[56:59], v[76:79], v[196:199]\n"
    "s_waitcnt lgkmcnt(5)\n"
    "v_mfma_f32_16x16x32_bf16 v[196:199], v[60:63], v[72:75], v[196:199]\n"
    "v_mfma_f32_16x16x32_bf16 v[200:203], v[56:59], v[80:83], v[200:203]\n"
    "v_mfma_f32_16x16x32_bf16 v[200:203], v[60:63], v[76:79], v[200:203]\n"
    "s_waitcnt lgkmcnt(3)\n"
    "v_mfma_f32_16x16x32_bf16 v[200:203], v[64:67], v[72:75], v[200:203]\n"
    "v_mfma_f32_16x16x32_bf16 v[212:215], v[56:59], v[92:95], v[212:215]\n"
    "v_mfma_f32_16x16x32_bf16 v[212:215], v[60:63], v[88:91], v[212:215]\n"
    "v_mfma_f32_16x16x32_bf16 v[212:215], v[64:67], v[84:87], v[212:215]\n"
    "s_waitcnt lgkmcnt(1)\n"
    "v_mfma_f32_16x16x32_bf16 v[212:215], v[68:71], v[80:83], v[212:215]\n"
    "v_mfma_f32_16x16x32_bf16 v[212:215], v[128:131], v[76:79], v[212:215]\n"
    "ds_read_b32 v161, v191\n"
    "v_mfma_f32_16x16x32_bf16 v[216:219], v[56:59], v[96:99], v[216:219]\n"
    "v_mfma_f32_16x16x32_bf16 v[216:219], v[60:63], v[92:95], v[216:219]\n"
    "v_mfma_f32_16x16x32_bf16 v[216:219], v[64:67], v[88:91], v[216:219]\n"
    "v_mfma_f32_16x16x32_bf16 v[216:219], v[68:71], v[84:87], v[216:219]\n"
    "v_mfma_f32_16x16x32_bf16 v[216:219], v[128:131], v[80:83], v[216:219]\n"
    "v_mfma_f32_16x16x32_bf16 v[216:219], v[104:107], v[120:123], v[216:219]\n"
    "s_waitcnt lgkmcnt(0)\n"
    "v_cmp_gt_u32 vcc, 2, v161\n"
    "v_mov_b32 v162, 3\n"
    "s_and_b64 vcc, exec, vcc\n"
    "s_cbranch_vccz .Lk_cp0\n"
    "s_mov_b32 s69, 4194304\n"
    ".Lk_l28:\n"
    "ds_read_b32 v161, v191\n"
    "s_waitcnt lgkmcnt(0)\n"
    "v_cmp_gt_u32 vcc, 2, v161\n"
    "s_and_b64 vcc, exec, vcc\n"
    "s_cbranch_vccz .Lk_l29\n"
    "s_sleep 1\n"
    "s_sub_u32 s69, s69, 1\n"
    "s_cbranch_scc0 .Lk_l28\n"
    ".Lk_l29:\n"
    ".Lk_cp0:\n"
    "v_mfma_f32_16x16x32_bf16 v[220:223], v[56:59], v[100:103], v[220:223]\n"
    "ds_write_b128 v156, v[196:199] offset:0\n"
    "v_mfma_f32_16x16x32_bf16 v[220:223], v[60:63], v[96:99], v[220:223]\n"
    "ds_write_b128 v156, v[200:203] offset:1024\n"
    "v_mfma_f32_16x16x32_bf16 v[220:223], v[64:67], v[92:95], v[220:223]\n"
    "ds_write_b128 v156, v[212:215] offset:2048\n"
    "v_mfma_f32_16x16x32_bf16 v[220:223], v[68:71], v[88:91], v[220:223]\n"
    "ds_write_b128 v156, v[216:219] offset:3072\n"
    "v_mfma_f32_16x16x32_bf16 v[220:223], v[128:131], v[84:87], v[220:223]\n"
    "ds_write_b32 v165, v162\n"
    "v_mfma_f32_16x16x32_bf16 v[220:223], v[104:107], v[124:127], v[220:223]\n"
    "v_mfma_f32_16x16x32_bf16 v[220:223], v[108:111], v[120:123], v[220:223]\n"
    "v_mfma_f32_16x16x32_bf16 v[208:211], v[56:59], v[88:91], v[208:211]\n"
    "v_mfma_f32_16x16x32_bf16 v[208:211], v[60:63], v[84:87], v[208:211]\n"
    "v_mfma_f32_16x16x32_bf16 v[208:211], v[64:67], v[80:83], v[208:211]\n"
    "v_mfma_f32_16x16x32_bf16 v[208:211], v[68:71], v[76:79], v[208:211]\n"
    "v_mfma_f32_16x16x32_bf16 v[208:211], v[128:131], v[72:75], v[208:211]\n"
    "v_mfma_f32_16x16x32_bf16 v[204:207], v[56:59], v[84:87], v[204:207]\n"
    "ds_read_b32 v161, v158\n"
    "v_mfma_f32_16x16x32_bf16 v[204:207], v[60:63], v[80:83], v[204:207]\n"
    "ds_read_b128 v[8:11], v157\n"
    "v_mfma_f32_16x16x32_bf16 v[204:207], v[64:67], v[76:79], v[204:207]\n"
    "ds_read_b128 v[12:15], v157 offset:1024\n"
    "v_mfma_f32_16x16x32_bf16 v[204:207], v[68:71], v[72:75], v[204:207]\n"
    "ds_read_b128 v[16:19], v157 offset:2048\n"
    "v_mfma_f32_16x16x32_bf16 v[192:195], v[56:59], v[72:75], v[192:195]\n"
    "ds_read_b128 v[20:23], v157 offset:3072\n"
    "s_setprio 3\n"
    "s_waitcnt lgkmcnt(4)\n"
    "v_cmp_gt_u32 vcc, 3, v161\n"
    "s_and_b64 vcc, exec, vcc\n"
    "s_cbranch_vccz .Lk_cq0\n"
    "s_mov_b32 s69, 4194304\n"
    ".Lk_l30:\n"
    "ds_read_b32 v161, v158\n"
    "s_waitcnt lgkmcnt(0)\n"
    "v_cmp_gt_u32 vcc, 3, v161\n"
    "s_and_b64 vcc, exec, vcc\n"
    "s_cbranch_vccz .Lk_l31\n"
    "s_sleep 1\n"
    "s_sub_u32 s69, s69, 1\n"
    "s_cbranch_scc0 .Lk_l30\n"
    ".Lk_l31:\n"
    "ds_read_b128 v[8:11], v157\n"
    "ds_read_b128 v[12:15], v157 offset:1024\n"
    "ds_read_b128 v[16:19], v157 offset:2048\n"
    "ds_read_b128 v[20:23], v157 offset:3072\n"
    ".Lk_cq0:\n"
    "s_waitcnt lgkmcnt(3)\n"
    "s_nop 0\n"
    "v_add_f32 v192, v192, v8\n"
    "v_add_f32 v193, v193, v9\n"
    "v_add_f32 v194, v194, v10\n"
    "v_add_f32 v195, v195, v11\n"
    "v_cvt_pk_bf16_f32 v168, v192, v193\n"
    "v_cvt_pk_bf16_f32 v169, v194, v195\n"
    "s_waitcnt lgkmcnt(2)\n"
    "v_add_f32 v204, v204, v12\n"
    "v_add_f32 v205, v205, v13\n"
    "v_add_f32 v206, v206, v14\n"
    "v_add_f32 v207, v207, v15\n"
    "v_cvt_pk_bf16_f32 v170, v204, v205\n"
    "v_cvt_pk_bf16_f32 v171, v206, v207\n"
    "s_waitcnt lgkmcnt(1)\n"
    "v_add_f32 v208, v208, v16\n"
    "v_add_f32 v209, v209, v17\n"
    "v_add_f32 v210, v210, v18\n"
    "v_add_f32 v211, v211, v19\n"
    "v_cvt_pk_bf16_f32 v172, v208, v209\n"
    "v_cvt_pk_bf16_f32 v173, v210, v211\n"
    "s_waitcnt lgkmcnt(0)\n"
    "v_add_f32 v220, v220, v20\n"
    "v_add_f32 v221, v221, v21\n"
    "v_add_f32 v222, v222, v22\n"
    "v_add_f32 v223, v223, v23\n"
    "v_cvt_pk_bf16_f32 v174, v220, v221\n"
    "v_cvt_pk_bf16_f32 v175, v222, v223\n"
    "v_permlane16_swap_b32 v168, v170\n"
    "v_permlane16_swap_b32 v169, v171\n"
    "v_bfi_b32 v168, s55, v176, v168\n"
    "v_bfi_b32 v169, s55, v177, v169\n"
    "v_bfi_b32 v170, s55, v178, v170\n"
    "v_bfi_b32 v171, s55, v179, v171\n"
    "buffer_store_dwordx4 v[168:171], v159, s[12:15], s30 offen sc1\n"
    "v_permlane16_swap_b32 v172, v174\n"
    "v_permlane16_swap_b32 v173, v175\n"
    "v_bfi_b32 v172, s55, v176, v172\n"
    "v_bfi_b32 v173, s55, v177, v173\n"
    "v_bfi_b32 v174, s55, v178, v174\n"
    "v_bfi_b32 v175, s55, v179, v175\n"
    "buffer_store_dwordx4 v[172:175], v159, s[12:15], s30 offen offset:2048 sc1\n"
    "s_branch .Lk_cvx\n"
    ".Lk_cv1:\n"
    "s_waitcnt lgkmcnt(14)\n"
    "v_mfma_f32_16x16x32_bf16 v[192:195], v[56:59], v[72:75], v[192:195]\n"
    "s_waitcnt lgkmcnt(11)\n"
    "v_mfma_f32_16x16x32_bf16 v[204:207], v[56:59], v[84:87], v[204:207]\n"
    "s_waitcnt lgkmcnt(5)\n"
    "v_mfma_f32_16x16x32_bf16 v[204:207], v[60:63], v[80:83], v[204:207]\n"
    "s_waitcnt lgkmcnt(3)\n"
    "v_mfma_f32_16x16x32_bf16 v[204:207], v[64:67], v[76:79], v[204:207]\n"
    "s_waitcnt lgkmcnt(1)\n"
    "v_mfma_f32_16x16x32_bf16 v[204:207], v[68:71], v[72:75], v[204:207]\n"
    "v_mfma_f32_16x16x32_bf16 v[208:211], v[56:59], v[88:91], v[208:211]\n"
    "v_mfma_f32_16x16x32_bf16 v[208:211], v[60:63], v[84:87], v[208:211]\n"
    "v_mfma_f32_16x16x32_bf16 v[208:211], v[64:67], v[80:83], v[208:211]\n"
    "v_mfma_f32_16x16x32_bf16 v[208:211], v[68:71], v[76:79], v[208:211]\n"
    "v_mfma_f32_16x16x32_bf16 v[208:211], v[104:107], v[120:123], v[208:211]\n"
    "ds_read_b32 v161, v191\n"
    "v_mfma_f32_16x16x32_bf16 v[220:223], v[56:59], v[100:103], v[220:223]\n"
    "v_mfma_f32_16x16x32_bf16 v[220:223], v[60:63], v[96:99], v[220:223]\n"
    "v_mfma_f32_16x16x32_bf16 v[220:223], v[64:67], v[92:95], v[220:223]\n"
    "v_mfma_f32_16x16x32_bf16 v[220:223], v[68:71], v[88:91], v[220:223]\n"
    "v_mfma_f32_16x16x32_bf16 v[220:223], v[104:107], v[132:135], v[220:223]\n"
    "v_mfma_f32_16x16x32_bf16 v[220:223], v[108:111], v[128:131], v[220:223]\n"
    "s_waitcnt lgkmcnt(0)\n"
    "v_cmp_gt_u32 vcc, 2, v161\n"
    "v_mov_b32 v162, 3\n"
    "s_and_b64 vcc, exec, vcc\n"
    "s_cbranch_vccz .Lk_cp1\n"
    "s_mov_b32 s69, 4194304\n"
    ".Lk_l32:\n"
    "ds_read_b32 v161, v191\n"
    "s_waitcnt lgkmcnt(0)\n"
    "v_cmp_gt_u32 vcc, 2, v161\n"
    "s_and_b64 vcc, exec, vcc\n"
    "s_cbranch_vccz .Lk_l33\n"
    "s_sleep 1\n"
    "s_sub_u32 s69, s69, 1\n"
    "s_cbranch_scc0 .Lk_l32\n"
    ".Lk_l33:\n"
    ".Lk_cp1:\n"
    "v_mfma_f32_16x16x32_bf16 v[216:219], v[56:59], v[96:99], v[216:219]\n"
    "ds_write_b128 v156, v[192:195] offset:0\n"
    "v_mfma_f32_16x16x32_bf16 v[216:219], v[60:63], v[92:95], v[216:219]\n"
    "ds_write_b128 v156, v[204:207] offset:1024\n"
    "v_mfma_f32_16x16x32_bf16 v[216:219], v[64:67], v[88:91], v[216:219]\n"
    "ds_write_b128 v156, v[208:211] offset:2048\n"
    "v_mfma_f32_16x16x32_bf16 v[216:219], v[68:71], v[84:87], v[216:219]\n"
    "ds_write_b128 v156, v[220:223] offset:3072\n"
    "v_mfma_f32_16x16x32_bf16 v[216:219], v[104:107], v[128:131], v[216:219]\n"
    "ds_write_b32 v165, v162\n"
    "v_mfma_f32_16x16x32_bf16 v[216:219], v[108:111], v[124:127], v[216:219]\n"
    "v_mfma_f32_16x16x32_bf16 v[212:215], v[56:59], v[92:95], v[212:215]\n"
    "v_mfma_f32_16x16x32_bf16 v[212:215], v[60:63], v[88:91], v[212:215]\n"
    "v_mfma_f32_16x16x32_bf16 v[212:215], v[64:67], v[84:87], v[212:215]\n"
    "v_mfma_f32_16x16x32_bf16 v[212:215], v[68:71], v[80:83], v[212:215]\n"
    "v_mfma_f32_16x16x32_bf16 v[212:215], v[104:107], v[124:127], v[212:215]\n"
    "v_mfma_f32_16x16x32_bf16 v[212:215], v[108:111], v[120:123], v[212:215]\n"
    "v_mfma_f32_16x16x32_bf16 v[200:203], v[56:59], v[80:83], v[200:203]\n"
    "ds_read_b32 v161, v158\n"
    "v_mfma_f32_16x16x32_bf16 v[200:203], v[60:63], v[76:79], v[200:203]\n"
    "ds_read_b128 v[8:11], v157\n"
    "v_mfma_f32_16x16x32_bf16 v[200:203], v[64:67], v[72:75], v[200:203]\n"
    "ds_read_b128 v[12:15], v157 offset:1024\n"
    "v_mfma_f32_16x16x32_bf16 v[196:199], v[56:59], v[76:79], v[196:199]\n"
    "ds_read_b128 v[16:19], v157 offset:2048\n"
    "v_mfma_f32_16x16x32_bf16 v[196:199], v[60:63], v[72:75], v[196:199]\n"
    "ds_read_b128 v[20:23], v157 offset:3072\n"
    "s_setprio 3\n"
    "s_waitcnt lgkmcnt(4)\n"
    "v_cmp_gt_u32 vcc, 3, v161\n"
    "s_and_b64 vcc, exec, vcc\n"
    "s_cbranch_vccz .Lk_cq1\n"
    "s_mov_b32 s69, 4194304\n"
    ".Lk_l34:\n"
    "ds_read_b32 v161, v158\n"
    "s_waitcnt lgkmcnt(0)\n"
    "v_cmp_gt_u32 vcc, 3, v161\n"
    "s_and_b64 vcc, exec, vcc\n"
    "s_cbranch_vccz .Lk_l35\n"
    "s_sleep 1\n"
    "s_sub_u32 s69, s69, 1\n"
    "s_cbranch_scc0 .Lk_l34\n"
    ".Lk_l35:\n"
    "ds_read_b128 v[8:11], v157\n"
    "ds_read_b128 v[12:15], v157 offset:1024\n"
    "ds_read_b128 v[16:19], v157 offset:2048\n"
    "ds_read_b128 v[20:23], v157 offset:3072\n"
    ".Lk_cq1:\n"
    "s_waitcnt lgkmcnt(3)\n"
    "s_nop 0\n"
    "v_add_f32 v196, v196, v8\n"
    "v_add_f32 v197, v197, v9\n"
    "v_add_f32 v198, v198, v10\n"
    "v_add_f32 v199, v199, v11\n"
    "v_cvt_pk_bf16_f32 v168, v196, v197\n"
    "v_cvt_pk_bf16_f32 v169, v198, v199\n"
    "s_waitcnt lgkmcnt(2)\n"
    "v_add_f32 v200, v200, v12\n"
    "v_add_f32 v201, v201, v13\n"
    "v_add_f32 v202, v202, v14\n"
    "v_add_f32 v203, v203, v15\n"
    "v_cvt_pk_bf16_f32 v170, v200, v201\n"
    "v_cvt_pk_bf16_f32 v171, v202, v203\n"
    "s_waitcnt lgkmcnt(1)\n"
    "v_add_f32 v212, v212, v16\n"
    "v_add_f32 v213, v213, v17\n"
    "v_add_f32 v214, v214, v18\n"
    "v_add_f32 v215, v215, v19\n"
    "v_cvt_pk_bf16_f32 v172, v212, v213\n"
    "v_cvt_pk_bf16_f32 v173, v214, v215\n"
    "s_waitcnt lgkmcnt(0)\n"
    "v_add_f32 v216, v216, v20\n"
    "v_add_f32 v217, v217, v21\n"
    "v_add_f32 v218, v218, v22\n"
    "v_add_f32 v219, v219, v23\n"
    "v_cvt_pk_bf16_f32 v174, v216, v217\n"
    "v_cvt_pk_bf16_f32 v175, v218, v219\n"
    "v_permlane16_swap_b32 v168, v170\n"
    "v_permlane16_swap_b32 v169, v171\n"
    "v_bfi_b32 v168, s55, v176, v168\n"
    "v_bfi_b32 v169, s55, v177, v169\n"
    "v_bfi_b32 v170, s55, v178, v170\n"
    "v_bfi_b32 v171, s55, v179, v171\n"
    "buffer_store_dwordx4 v[168:171], v159, s[12:15], s30 offen sc1\n"
    "v_permlane16_swap_b32 v172, v174\n"
    "v_permlane16_swap_b32 v173, v175\n"
    "v_bfi_b32 v172, s55, v176, v172\n"
    "v_bfi_b32 v173, s55, v177, v173\n"
    "v_bfi_b32 v174, s55, v178, v174\n"
    "v_bfi_b32 v175, s55, v179, v175\n"
    "buffer_store_dwordx4 v[172:175], v159, s[12:15], s30 offen offset:2048 sc1\n"
    ".Lk_cvx:\n"
    "v_mov_b32 v161, 4\n"
    "ds_write_b32 v165, v161\n"
    "v_readfirstlane_b32 s66, v166\n"
    "v_readfirstlane_b32 s67, v167\n"
    "s_waitcnt lgkmcnt(0)\n"
    "s_xor_b32 s50, s40, 4\n"
    "s_xor_b32 s51, s45, 15\n"
    "s_or_b32 s50, s50, s51\n"
    "s_or_b32 s50, s50, s44\n"
    "s_cbranch_scc1 .Lk_nobump\n"
    "v_lshl_add_u32 v8, v184, 5, v185\n"
    "v_lshlrev_b32 v8, 7, v8\n"
    "s_mov_b32 s69, 65536\n"
    ".Lk_bump:\n"
    "s_mov_b32 s50, 16384\n"
    "buffer_load_dwordx2 v[10:11], v8, s[12:15], s50 offen sc1\n"
    "s_mov_b32 s50, 32768\n"
    "buffer_load_dwordx2 v[12:13], v8, s[12:15], s50 offen sc1\n"
    "s_mov_b32 s50, 49152\n"
    "buffer_load_dwordx2 v[14:15], v8, s[12:15], s50 offen sc1\n"
    "s_mov_b32 s50, 65536\n"
    "buffer_load_dwordx2 v[16:17], v8, s[12:15], s50 offen sc1\n"
    "s_waitcnt vmcnt(3)\n"
    "v_cmp_ne_u64 s[50:51], s[66:67], v[10:11]\n"
    "s_waitcnt vmcnt(2)\n"
    "v_cmp_ne_u64 s[52:53], s[66:67], v[12:13]\n"
    "s_waitcnt vmcnt(1)\n"
    "v_cmp_ne_u64 s[54:55], s[66:67], v[14:15]\n"
    "s_waitcnt vmcnt(0)\n"
    "v_cmp_ne_u64 s[56:57], s[66:67], v[16:17]\n"
    "s_or_b64 s[50:51], s[50:51], s[52:53]\n"
    "s_or_b64 s[54:55], s[54:55], s[56:57]\n"
    "s_or_b64 s[50:51], s[50:51], s[54:55]\n"
    "s_cbranch_scc0 .Lk_bump2\n"
    "s_sub_u32 s69, s69, 1\n"
    "s_cbranch_scc0 .Lk_bump\n"
    ".Lk_bump2:\n"
    "s_mov_b64 exec, 1\n"
    "buffer_store_dwordx2 v[166:167], off, s[12:15], 0 sc1\n"
    "s_mov_b64 exec, -1\n"
    ".Lk_nobump:\n"
    "v_lshrrev_b32 v8, 3, v1\n"
    "v_and_b32 v9, 7, v1\n"
    "v_lshlrev_b32 v9, 4, v9\n"
    "v_lshl_add_u32 v8, v8, 12, v9\n"
    "s_lshl2_add_u32 s50, s47, s40\n"
    "s_lshl_b32 s50, s50, 16\n"
    "s_lshl_b32 s51, s46, 7\n"
    "s_add_u32 s50, s50, s51\n"
    "s_add_u32 s60, s50, 262144\n"
    "s_add_u32 s61, s60, 32768\n"
    "s_lshl_b32 s50, s40, 11\n"
    "s_add_u32 s50, s50, 135232\n"
    "v_lshl_add_u32 v9, v1, 4, s50\n"
    "v_and_b32 v10, 7, v1\n"
    "v_lshlrev_b32 v10, 2, v10\n"
    "v_mov_b32 v11, 1145359564\n"
    "v_lshrrev_b32 v11, v10, v11\n"
    "v_and_b32 v11, 15, v11\n"
    "v_add_u32 v10, 163808, v10\n"
    "s_mov_b32 s69, 4194304\n"
    ".Lk_l36:\n"
    "ds_read_b32 v161, v10\n"
    "s_waitcnt lgkmcnt(0)\n"
    "v_cmp_gt_u32 vcc, v11, v161\n"
    "s_and_b64 vcc, exec, vcc\n"
    "s_cbranch_vccz .Lk_l37\n"
    "s_sleep 1\n"
    "s_sub_u32 s69, s69, 1\n"
    "s_cbranch_scc0 .Lk_l36\n"
    ".Lk_l37:\n"
    "s_mov_b32 s69, 65536\n"
    ".Lk_ard:\n"
    "buffer_load_dwordx4 v[12:15], v8, s[12:15], s60 offen sc1\n"
    "buffer_load_dwordx4 v[16:19], v8, s[12:15], s61 offen sc1\n"
    "s_waitcnt vmcnt(1)\n"
    "v_xor_b32 v12, v176, v12\n"
    "v_xor_b32 v13, v177, v13\n"
    "v_or3_b32 v28, v12, v13, 0\n"
    "v_xor_b32 v14, v178, v14\n"
    "v_xor_b32 v15, v179, v15\n"
    "v_or3_b32 v28, v14, v15, v28\n"
    "s_waitcnt vmcnt(0)\n"
    "v_xor_b32 v16, v176, v16\n"
    "v_xor_b32 v17, v177, v17\n"
    "v_or3_b32 v28, v16, v17, v28\n"
    "v_xor_b32 v18, v178, v18\n"
    "v_xor_b32 v19, v179, v19\n"
    "v_or3_b32 v28, v18, v19, v28\n"
    "v_and_b32 v28, 1073758208, v28\n"
    "v_cmp_ne_u32 vcc, 0, v28\n"
    "s_and_b64 vcc, exec, vcc\n"
    "s_cbranch_vccz .Lk_aok\n"
    "s_sleep 2\n"
    "s_sub_u32 s69, s69, 1\n"
    "s_cbranch_scc0 .Lk_ard\n"
    ".Lk_aok:\n"
    "ds_write_b128 v9, v[12:15] offset:0\n"
    "ds_write_b128 v9, v[16:19] offset:1024\n"
    "v_mov_b32 v161, 5\n"
    "ds_write_b32 v165, v161\n"
    "s_endpgm\n"
    ".Lk_end:\n"
    :: "{s[0:1]}"(__builtin_amdgcn_kernarg_segment_ptr()), "{s2}"(blockIdx.x), "{v0}"(threadIdx.x)
    : "memory", "vcc", "scc", "v1", "v2", "v3", "v4", "v5", "v6", "v7", "v8", "v9", "v10", "v11", "v12", "v13", "v14", "v15", "v16", "v17", "v18", "v19", "v20", "v21", "v22", "v23", "v24", "v25", "v26", "v27", "v28", "v29", "v30", "v31", "v32", "v33", "v34", "v35", "v36", "v37", "v38", "v39", "v40", "v41", "v42", "v43", "v44", "v45", "v46", "v47", "v48", "v49", "v50", "v51", "v52", "v53", "v54", "v55", "v56", "v57", "v58", "v59", "v60", "v61", "v62", "v63", "v64", "v65", "v66", "v67", "v68", "v69", "v70", "v71", "v72", "v73", "v74", "v75", "v76", "v77", "v78", "v79", "v80", "v81", "v82", "v83", "v84", "v85", "v86", "v87", "v88", "v89", "v90", "v91", "v92", "v93", "v94", "v95", "v96", "v97", "v98", "v99", "v100", "v101", "v102", "v103", "v104", "v105", "v106", "v107", "v108", "v109", "v110", "v111", "v112", "v113", "v114", "v115", "v116", "v117", "v118", "v119", "v120", "v121", "v122", "v123", "v124", "v125", "v126", "v127", "v128", "v129", "v130", "v131", "v132", "v133", "v134", "v135", "v136", "v137", "v138", "v139", "v140", "v141", "v142", "v143", "v144", "v145", "v146", "v147", "v148", "v149", "v150", "v151", "v152", "v153", "v154", "v155", "v156", "v157", "v158", "v159", "v160", "v161", "v162", "v163", "v164", "v165", "v166", "v167", "v168", "v169", "v170", "v171", "v172", "v173", "v174", "v175", "v176", "v177", "v178", "v179", "v180", "v181", "v182", "v183", "v184", "v185", "v186", "v187", "v188", "v189", "v190", "v191", "v192", "v193", "v194", "v195", "v196", "v197", "v198", "v199", "v200", "v201", "v202", "v203", "v204", "v205", "v206", "v207", "v208", "v209", "v210", "v211", "v212", "v213", "v214", "v215", "v216", "v217", "v218", "v219", "v220", "v221", "v222", "v223", "v224", "v225", "v226", "v227", "v228", "v229", "v230", "v231", "v232", "v233", "v234", "v235", "v236", "v237", "v238", "v239", "v240", "v241", "v242", "v243", "v244", "v245", "v246", "v247", "v248", "v249", "v250", "v251", "v252", "v253", "v254", "v255", "s3", "s4", "s5", "s6", "s7", "s8", "s9", "s10", "s11", "s12", "s13", "s14", "s15", "s16", "s17", "s18", "s19", "s20", "s21", "s22", "s23", "s24", "s25", "s26", "s27", "s28", "s29", "s30", "s31", "s33", "s34", "s35", "s36", "s37", "s38", "s39", "s40", "s41", "s42", "s43", "s44", "s45", "s46", "s47", "s48", "s49", "s50", "s51", "s52", "s53", "s54", "s55", "s56", "s57", "s58", "s59", "s60", "s61", "s62", "s63", "s64", "s65", "s66", "s67", "s68", "s69", "s70", "s71", "s72", "s73", "s74", "s75", "s76", "s77", "s78", "s79", "s80", "s81", "s82", "s83", "s84", "s85", "s86", "s87", "s88", "s89", "s90", "s91", "s92", "s93", "s94", "s95");
}

extern "C" void kernel_launch(void* const* d_in, const int* in_sizes, int n_in, void* d_out, int out_size, void* d_ws, size_t ws_size,
                              hipStream_t stream) {
  (void)hipFuncSetAttribute((const void*)fused_kernel, hipFuncAttributeMaxDynamicSharedMemorySize, 163840);
  const unsigned F = 0x00020000u;
  const Args a = {{d_in[0], 2048 * 512 * 2, F}, {d_in[8], 1376 * 512 * 2, F}, {(char*)d_ws + 0u, WS_END, F}, {d_in[9], 1376 * 512 * 2, F},
                  {d_in[10], 1376 * 512 * 2, F}, {d_in[1], d_in[2], d_in[3], d_in[4], d_in[5], d_in[6]},
                  {d_out, 2048 * 512 * 2, F}, {d_in[11], 1024, F}};
  fused_kernel<<<256, 512, 163840, stream>>>(a);
}
```
